```python
import torch

def _softmax(x, axis=-1):
    return torch.softmax(x, dim=axis)
def _gelu(x):
    return torch.nn.functional.gelu(x, approximate="tanh")
def _silu(x):
    return torch.nn.functional.silu(x)
def _sigmoid(x):
    return torch.sigmoid(x)
def _layernorm(x, axis=-1, eps=1e-5):
    m = x.mean(dim=axis, keepdim=True)
    v = x.var(dim=axis, keepdim=True, unbiased=False)
    return (x - m) / torch.sqrt(v + eps)
def _scatter_add(tgt, idx, src, axis=0):
    return tgt.clone().index_add_(axis, idx, src)
def _segment_sum(src, seg, n):
    out = torch.zeros((n,) + src.shape[1:], dtype=src.dtype)
    return out.index_add_(0, seg, src)
def _pad(x, pw):
    flat = tuple(v for pair in reversed(pw) for v in pair)
    return torch.nn.functional.pad(x, flat)

kernel_name = 'concat_heads__sliding_local_dilated__mha_block__prompt'

def setup_inputs():

    _w = (lambda a: (lambda t: t.to(torch.bfloat16) if t.is_floating_point() else t)(torch.as_tensor(a)))
    def _A_setup():
        B, S, D = 1, 2048, 448
        return {
            "x": _w((lambda *s: torch.randn(*s, dtype=torch.bfloat16) * 0.4)(B, S, D)),
            "wqkv": _w((lambda *s: torch.randn(*s, dtype=torch.bfloat16) * 0.8)(D, 3 * D)),
            "wo": _w((lambda *s: torch.randn(*s, dtype=torch.bfloat16) * 0.1)(D, D)),
        }
    _A_in = _A_setup()

    def _B_setup():
        B, S, D, H = 1, 2048, 448, 16
        return {
            "x": _w((lambda *s: torch.randn(*s, dtype=torch.bfloat16) * 0.2)(B, S, D)),
            "wqkv": _w((lambda *s: torch.randn(*s, dtype=torch.bfloat16) * 0.8)(D, 3 * D)),
            "wo": _w((lambda *s: torch.randn(*s, dtype=torch.bfloat16) * 0.2)(D, D)),
        }
    _B_in = _B_setup()

    return {
        "A_x": _A_in["x"],
        "A_wqkv": _A_in["wqkv"],
        "A_wo": _A_in["wo"],
        "B_x": _B_in["x"],
        "B_wqkv": _B_in["wqkv"],
        "B_wo": _B_in["wo"],
    }

def reference(A_x, A_wqkv, A_wo, B_x, B_wqkv, B_wo):
    def _A_ref(x, wqkv, wo):
        B, S, D = x.shape
        H = 16; Dh = D // H
        W, dil = 8, 2
        qkv = (lambda x, *d: x.permute(*d))((x @ wqkv).reshape(B, S, 3, H, Dh), 2, 0, 3, 1, 4)
        q, k, v = qkv[0], qkv[1], qkv[2]
        scale = Dh ** -0.5
        pos = torch.arange(S)
        off = torch.arange(W) * dil
        idx = pos[:, None] - off[None, :]
        mask = idx >= 0
        idx_c = torch.where(mask, idx, 0).reshape(-1)
        kg = k[:, :, idx_c, :].reshape(B, H, S, W, Dh)
        vg = v[:, :, idx_c, :].reshape(B, H, S, W, Dh)
        logits = torch.einsum("bhsd,bhswd->bhsw", q, kg) * scale
        logits = torch.where(mask[None, None, :, :], logits, -1e9)
        attn = _softmax(logits, axis=-1)
        o = torch.einsum("bhsw,bhswd->bhsd", attn, vg)
        return (lambda x, *d: x.permute(*d))(o, 0, 2, 1, 3).reshape(B, S, D) @ wo

    def _B_ref(x, wqkv, wo):
        B, S, D = x.shape
        H = 16
        Dh = D // H
        qkv = (lambda x, *d: x.permute(*d))((x @ wqkv).reshape(B, S, 3, H, Dh), 2, 0, 3, 1, 4)
        q, k, v = qkv[0], qkv[1], qkv[2]
        scores = torch.einsum("bhqd,bhkd->bhqk", q, k) * (Dh ** -0.5)
        p = _softmax(scores, axis=-1)
        o = torch.einsum("bhqk,bhkd->bhqd", p, v)
        return (lambda x, *d: x.permute(*d))(o, 0, 2, 1, 3).reshape(B, S, D) @ wo

    a = _A_ref(A_x, A_wqkv, A_wo)
    b = _B_ref(B_x, B_wqkv, B_wo)
    return torch.concatenate([a, b], axis=-1)
```

```cpp
#include <hip/hip_runtime.h>
#include <hip/hip_bf16.h>
typedef __hip_bfloat16 bf16;
#define S_ 2048
#define D_ 448
#define H_ 16
#define DH 28
__device__ inline float b2f(bf16 v){ return __bfloat162float(v);}
__global__ void gemm_naive(const bf16* A, const bf16* B, bf16* C, int M, int N, int K, int ldc){
  int n = blockIdx.x*64+threadIdx.x; int m = blockIdx.y;
  if(n>=N) return;
  float acc=0; for(int k=0;k<K;k++) acc += b2f(A[m*K+k])*b2f(B[k*N+n]);
  C[m*ldc+n]=__float2bfloat16(acc);
}
__global__ void attnA(const bf16* qkv, bf16* o){
  int s = blockIdx.x*64+threadIdx.x; int h = blockIdx.y;
  float lg[8]; float m=-1e30f;
  const float scale = rsqrtf(28.f);
  #pragma unroll
  for(int w=0;w<8;w++){ int j=s-2*w; float a=0; if(j>=0){ for(int d=0;d<DH;d++) a+=b2f(qkv[s*1344+h*DH+d])*b2f(qkv[j*1344+448+h*DH+d]); a*=scale;} else a=-1e9f; lg[w]=a; m=fmaxf(m,a);}
  float l=0;
  #pragma unroll
  for(int w=0;w<8;w++){ lg[w]=expf(lg[w]-m); l+=lg[w];}
  for(int d=0;d<DH;d++){ float a=0;
  #pragma unroll
    for(int w=0;w<8;w++){ int j=s-2*w; if(j<0) j=0; a+=lg[w]*b2f(qkv[j*1344+896+h*DH+d]);}
    o[s*448+h*DH+d]=__float2bfloat16(a/l);}
}
__global__ void attnB(const bf16* qkv, bf16* o){
  int s = blockIdx.x*64+threadIdx.x; int h = blockIdx.y;
  float q[DH], acc[DH]; const float scale = rsqrtf(28.f);
  #pragma unroll
  for(int d=0;d<DH;d++){ q[d]=b2f(qkv[s*1344+h*DH+d]); acc[d]=0;}
  float m=-1e30f,l=0;
  for(int j=0;j<S_;j++){ float a=0;
  #pragma unroll
    for(int d=0;d<DH;d++) a+=q[d]*b2f(qkv[j*1344+448+h*DH+d]);
    a*=scale; float mn=fmaxf(m,a); float c=expf(m-mn), p=expf(a-mn); l=l*c+p; m=mn;
  #pragma unroll
    for(int d=0;d<DH;d++) acc[d]=acc[d]*c+p*b2f(qkv[j*1344+896+h*DH+d]);
  }
  #pragma unroll
  for(int d=0;d<DH;d++) o[s*448+h*DH+d]=__float2bfloat16(acc[d]/l);
}
extern "C" void kernel_launch(void* const* d_in, const int* in_sizes, int n_in, void* d_out, int out_size, void* d_ws, size_t ws_size, hipStream_t stream) {
  const bf16* Ax=(const bf16*)d_in[0]; const bf16* Aw=(const bf16*)d_in[1]; const bf16* Ao=(const bf16*)d_in[2];
  const bf16* Bx=(const bf16*)d_in[3]; const bf16* Bw=(const bf16*)d_in[4]; const bf16* Bo=(const bf16*)d_in[5];
  bf16* ws=(bf16*)d_ws; bf16* qa=ws; bf16* qb=qa+S_*1344; bf16* oa=qb+S_*1344; bf16* ob=oa+S_*448;
  bf16* out=(bf16*)d_out;
  gemm_naive<<<dim3(21,S_),64,0,stream>>>(Ax,Aw,qa,S_,1344,448,1344);
  gemm_naive<<<dim3(21,S_),64,0,stream>>>(Bx,Bw,qb,S_,1344,448,1344);
  attnA<<<dim3(32,16),64,0,stream>>>(qa,oa);
  attnB<<<dim3(32,16),64,0,stream>>>(qb,ob);
  gemm_naive<<<dim3(7,S_),64,0,stream>>>(oa,Ao,out,S_,448,448,896);
  gemm_naive<<<dim3(7,S_),64,0,stream>>>(ob,Bo,out+448,S_,448,448,896);
}
```

```cpp
#include <hip/hip_runtime.h>
#include <stdint.h>

__global__ void __launch_bounds__(512, 2) mk_kernel(const void* xB, const void* wB, const void* xA, const void* wA, const void* woA, const void* woB, void* out, void* ws) {
#if defined(__HIP_DEVICE_COMPILE__)
  __shared__ __attribute__((aligned(128))) char smem[162112];
  asm volatile(
      ".Lmk_start:\n"
      "s_bfe_u32 s30, s2, 0x10003\n"
      "s_lshl_b32 s40, s30, 4\n"
      "s_load_dwordx2 s[4:5], s[0:1], s40\n"
      "s_or_b32 s41, s40, 8\n"
      "s_load_dwordx2 s[8:9], s[0:1], s41\n"
      "s_load_dwordx8 s[20:27], s[0:1], 0x20\n"
      "v_readfirstlane_b32 s3, v0\n"
      "v_and_b32 v1, 63, v0\n"
      "s_mov_b32 s6, 1835008\n"
      "s_mov_b32 s7, 0x20000\n"
      "s_mov_b32 s10, 1204224\n"
      "s_mov_b32 s11, s7\n"
      "s_lshr_b32 s3, s3, 6\n"
      "s_bfe_u32 s40, s2, 0x20004\n"
      "s_lshr_b32 s32, s2, 6\n"
      "s_bfe_u32 s41, s2, 0x20001\n"
      "s_lshl2_add_u32 s33, s41, s40\n"
      "s_and_b32 s41, s2, 1\n"
      "s_lshl2_add_u32 s34, s41, s32\n"
      "s_lshl_b32 s41, s34, 2\n"
      "s_lshr_b32 s41, 1696691248, s41\n"
      "s_and_b32 s41, s41, 7\n"
      "s_cmp_eq_u32 s30, 0\n"
      "s_cselect_b32 s34, s41, s34\n"
      "s_lshr_b32 s31, s2, 4\n"
      "s_cmp_lt_u32 s3, 4\n"
      "s_cbranch_scc1 .Lmk_main\n"
      "s_add_u32 s41, s40, s32\n"
      ".Lmk_ordb:\n"
      "s_mov_b32 s58, 106181136\n"
      "s_bitcmp1_b32 s41, 0\n"
      "s_cselect_b32 s58, 52843521, s58\n"
      ".Lmk_orde:\n"
      "s_and_b32 s37, s58, 15\n"
      "s_sub_u32 s38, s3, 4\n"
      "s_lshl_b32 s39, 896, s30\n"
      "v_lshrrev_b32 v10, 3, v1\n"
      "v_lshrrev_b32 v11, 4, v1\n"
      "v_and_b32 v12, 7, v1\n"
      "v_xor_b32 v11, v11, v12\n"
      "v_mul_u32_u24 v2, s39, v10\n"
      "v_lshl_add_u32 v2, v11, 4, v2\n"
      "v_xor_b32 v3, 64, v2\n"
      "s_lshl_b32 s40, s38, 5\n"
      "s_and_b32 s41, s40, 64\n"
      "s_and_b32 s54, s38, 1\n"
      "s_or_b32 s41, s41, s54\n"
      "s_cmp_eq_u32 s30, 0\n"
      "s_cselect_b32 s40, s40, s41\n"
      "s_cselect_b32 s44, 21, 7\n"
      "s_cselect_b32 s55, 0, 784\n"
      "s_lshl_b32 s41, s33, 7\n"
      "s_add_u32 s40, s40, s41\n"
      "s_mul_i32 s42, s40, 896\n"
      "s_lshl_b32 s43, s39, 3\n"
      "s_sub_u32 s43, s43, 1024\n"
      "s_lshl_b32 s48, s38, 12\n"
      "s_mov_b32 s47, 0\n"
      "s_lshl_b32 s40, s37, 7\n"
      "s_add_u32 s45, s42, s40\n"
      "s_add_u32 m0, s47, s48\n"
      "s_waitcnt lgkmcnt(0)\n"
      "buffer_load_dwordx4 v2, s[4:7], s45 offen offset:0 lds\n"
      "s_add_u32 s45, s45, s43\n"
      "buffer_load_dwordx4 v3, s[4:7], s45 offen offset:1024 lds\n"
      "s_add_u32 s45, s45, s43\n"
      "buffer_load_dwordx4 v2, s[4:7], s45 offen offset:2048 lds\n"
      "s_add_u32 s45, s45, s43\n"
      "buffer_load_dwordx4 v3, s[4:7], s45 offen offset:3072 lds\n"
      "s_load_dwordx2 s[28:29], s[26:27], 0x0\n"
      "s_mul_i32 s44, s44, s34\n"
      "s_lshl_b32 s44, s44, 4\n"
      "s_mul_i32 s46, s37, 172032\n"
      "s_add_u32 s46, s46, s44\n"
      "s_mul_i32 s49, s38, 5120\n"
      "s_add_u32 s49, s49, 18432\n"
      "s_mov_b32 m0, s49\n"
      "s_mul_i32 s40, s38, 320\n"
      "s_movk_i32 s56, 3121\n"
      "s_movk_i32 s57, 2688\n"
      "v_add_u32 v13, s40, v1\n"
      "v_mul_u32_u24 v14, s56, v13\n"
      "v_lshrrev_b32 v14, 16, v14\n"
      "v_mul_u32_u24 v15, 21, v14\n"
      "v_sub_u32 v15, v13, v15\n"
      "v_bfe_u32 v16, v14, 2, 2\n"
      "v_and_b32 v17, 3, v14\n"
      "v_lshl_or_b32 v16, v17, 2, v16\n"
      "v_and_or_b32 v16, v14, 48, v16\n"
      "v_mul_u32_u24 v17, 37, v15\n"
      "v_lshrrev_b32_e64 v17, 8, v17\n"
      "v_mul_u32_u24 v17, s55, v17\n"
      "v_lshl_add_u32 v17, v15, 4, v17\n"
      "v_mad_u32_u24 v4, v16, s57, v17\n"
      "buffer_load_dwordx4 v4, s[8:11], s46 offen offset:0 lds\n"
      "v_add_u32 v13, 64, v13\n"
      "v_mul_u32_u24 v14, s56, v13\n"
      "v_lshrrev_b32 v14, 16, v14\n"
      "v_mul_u32_u24 v15, 21, v14\n"
      "v_sub_u32 v15, v13, v15\n"
      "v_bfe_u32 v16, v14, 2, 2\n"
      "v_and_b32 v17, 3, v14\n"
      "v_lshl_or_b32 v16, v17, 2, v16\n"
      "v_and_or_b32 v16, v14, 48, v16\n"
      "v_mul_u32_u24 v17, 37, v15\n"
      "v_lshrrev_b32_e64 v17, 8, v17\n"
      "v_mul_u32_u24 v17, s55, v17\n"
      "v_lshl_add_u32 v17, v15, 4, v17\n"
      "v_mad_u32_u24 v5, v16, s57, v17\n"
      "v_subrev_u32 v5, 1024, v5\n"
      "buffer_load_dwordx4 v5, s[8:11], s46 offen offset:1024 lds\n"
      "v_add_u32 v13, 64, v13\n"
      "v_mul_u32_u24 v14, s56, v13\n"
      "v_lshrrev_b32 v14, 16, v14\n"
      "v_mul_u32_u24 v15, 21, v14\n"
      "v_sub_u32 v15, v13, v15\n"
      "v_bfe_u32 v16, v14, 2, 2\n"
      "v_and_b32 v17, 3, v14\n"
      "v_lshl_or_b32 v16, v17, 2, v16\n"
      "v_and_or_b32 v16, v14, 48, v16\n"
      "v_mul_u32_u24 v17, 37, v15\n"
      "v_lshrrev_b32_e64 v17, 8, v17\n"
      "v_mul_u32_u24 v17, s55, v17\n"
      "v_lshl_add_u32 v17, v15, 4, v17\n"
      "v_mad_u32_u24 v6, v16, s57, v17\n"
      "v_subrev_u32 v6, 2048, v6\n"
      "v_add_u32 v13, 64, v13\n"
      "v_mul_u32_u24 v14, s56, v13\n"
      "v_lshrrev_b32 v14, 16, v14\n"
      "v_mul_u32_u24 v15, 21, v14\n"
      "v_sub_u32 v15, v13, v15\n"
      "v_bfe_u32 v16, v14, 2, 2\n"
      "v_and_b32 v17, 3, v14\n"
      "v_lshl_or_b32 v16, v17, 2, v16\n"
      "v_and_or_b32 v16, v14, 48, v16\n"
      "v_mul_u32_u24 v17, 37, v15\n"
      "v_lshrrev_b32_e64 v17, 8, v17\n"
      "v_mul_u32_u24 v17, s55, v17\n"
      "v_lshl_add_u32 v17, v15, 4, v17\n"
      "v_mad_u32_u24 v7, v16, s57, v17\n"
      "v_subrev_u32 v7, 3072, v7\n"
      "v_add_u32 v13, 64, v13\n"
      "v_mul_u32_u24 v14, s56, v13\n"
      "v_lshrrev_b32 v14, 16, v14\n"
      "v_mul_u32_u24 v15, 21, v14\n"
      "v_sub_u32 v15, v13, v15\n"
      "v_bfe_u32 v16, v14, 2, 2\n"
      "v_and_b32 v17, 3, v14\n"
      "v_lshl_or_b32 v16, v17, 2, v16\n"
      "v_and_or_b32 v16, v14, 48, v16\n"
      "v_mul_u32_u24 v17, 37, v15\n"
      "v_lshrrev_b32_e64 v17, 8, v17\n"
      "v_mul_u32_u24 v17, s55, v17\n"
      "v_lshl_add_u32 v17, v15, 4, v17\n"
      "v_mad_u32_u24 v8, v16, s57, v17\n"
      "v_add_u32 v13, 1280, v1\n"
      "v_mul_u32_u24 v14, s56, v13\n"
      "v_lshrrev_b32 v14, 16, v14\n"
      "v_mul_u32_u24 v15, 21, v14\n"
      "v_sub_u32 v15, v13, v15\n"
      "v_bfe_u32 v16, v14, 2, 2\n"
      "v_and_b32 v17, 3, v14\n"
      "v_lshl_or_b32 v16, v17, 2, v16\n"
      "v_and_or_b32 v16, v14, 48, v16\n"
      "v_mul_u32_u24 v17, 37, v15\n"
      "v_lshrrev_b32 v17, 8, v17\n"
      "v_mul_u32_u24 v17, s55, v17\n"
      "v_lshl_add_u32 v17, v15, 4, v17\n"
      "v_mad_u32_u24 v9, v16, s57, v17\n"
      "v_add_u32 v9, s44, v9\n"
      "s_mov_b64 s[16:17], s[8:9]\n"
      "s_mov_b64 s[18:19], s[10:11]\n"
      "s_mov_b32 s50, 38912\n"
      "s_mov_b32 s51, 172032\n"
      "s_mov_b32 s53, 1\n"
      "s_cmp_eq_u32 s38, 0\n"
      "s_cbranch_scc1 .Lmk_ld_e0\n"
      "s_mov_b64 s[16:17], s[4:5]\n"
      "s_mov_b32 s18, s6\n"
      "s_add_u32 s50, s38, 15\n"
      "s_lshl_b32 s50, s50, 10\n"
      "s_movk_i32 s51, 128\n"
      "s_cmp_lt_u32 s38, 3\n"
      "s_cselect_b32 s53, s30, 0\n"
      "s_lshl_b32 s41, s33, 7\n"
      "s_add_u32 s41, s41, s38\n"
      "s_sub_u32 s41, s41, 17\n"
      "v_lshl_add_u32 v13, v10, 1, s41\n"
      "v_max_i32 v13, 0, v13\n"
      "v_mul_u32_u24 v13, 0x380, v13\n"
      "s_sub_u32 s41, s38, 1\n"
      "s_lshl_b32 s41, s41, 2\n"
      "v_xor_b32 v14, s41, v11\n"
      "v_and_b32 v14, 7, v14\n"
      "v_lshl_add_u32 v9, v14, 4, v13\n"
      ".Lmk_ld_e0:\n"
      "v_mov_b32 v13, 0\n"
      "v_and_b32 v14, 15, v1\n"
      "v_lshlrev_b32_e64 v14, 2, v14\n"
      "v_add_u32 v14, 161792, v14\n"
      "ds_write_b32 v14, v13\n"
      "s_mov_b32 s52, 6\n"
      ".Lmk_ld_loop:\n"
      "buffer_load_dwordx4 v6, s[8:11], s46 offen offset:2048 lds\n"
      "buffer_load_dwordx4 v7, s[8:11], s46 offen offset:3072 lds\n"
      "s_add_u32 m0, m0, 0x1000\n"
      "s_cmp_eq_u32 s53, 0\n"
      "buffer_load_dwordx4 v8, s[8:11], s46 offen offset:0 lds\n"
      "s_cbranch_scc1 .Lmk_ld_noe\n"
      "s_add_u32 m0, s47, s50\n"
      "s_mul_i32 s40, s37, s51\n"
      "buffer_load_dwordx4 v9, s[16:19], s40 offen lds\n"
      ".Lmk_ld_noe:\n"
      "s_sub_u32 s52, s52, 1\n"
      "s_cbranch_scc1 .Lmk_ld_done\n"
      "s_lshr_b32 s58, s58, 4\n"
      "s_and_b32 s37, s58, 15\n"
      "s_add_u32 s47, s47, 39936\n"
      "s_cmp_eq_u32 s47, 119808\n"
      "s_cselect_b32 s47, 0, s47\n"
      "s_lshl_b32 s40, s37, 7\n"
      "s_add_u32 s45, s42, s40\n"
      "s_add_u32 m0, s47, s48\n"
      "s_mul_i32 s46, s37, 172032\n"
      "buffer_load_dwordx4 v2, s[4:7], s45 offen offset:0 lds\n"
      "s_add_u32 s45, s45, s43\n"
      "buffer_load_dwordx4 v3, s[4:7], s45 offen offset:1024 lds\n"
      "s_add_u32 s45, s45, s43\n"
      "buffer_load_dwordx4 v2, s[4:7], s45 offen offset:2048 lds\n"
      "s_add_u32 s45, s45, s43\n"
      "buffer_load_dwordx4 v3, s[4:7], s45 offen offset:3072 lds\n"
      "s_add_u32 m0, s47, s49\n"
      "s_add_u32 s46, s46, s44\n"
      "buffer_load_dwordx4 v4, s[8:11], s46 offen offset:0 lds\n"
      "buffer_load_dwordx4 v5, s[8:11], s46 offen offset:1024 lds\n"
      "s_waitcnt vmcnt(6) lgkmcnt(0)\n"
      "s_barrier\n"
      "s_branch .Lmk_ld_loop\n"
      ".Lmk_ld_done:\n"
      "s_lshl2_add_u32 s40, s31, s38\n"
      "s_mul_i32 s40, s40, 49\n"
      "v_add_lshl_u32 v13, s40, v1, 7\n"
      "s_cmp_eq_u32 s30, 0\n"
      "s_cselect_b32 s41, 49, 0\n"
      "v_cmp_gt_u32 vcc, s41, v1\n"
      "v_mov_b32 v14, 2147483647\n"
      "s_mov_b32 m0, 161856\n"
      "s_mov_b64 s[4:5], s[20:21]\n"
      "s_mov_b64 s[8:9], s[22:23]\n"
      "s_mov_b32 s6, 401408\n"
      "s_mov_b32 s10, 401408\n"
      "v_cndmask_b32 v13, v14, v13, vcc\n"
      "buffer_load_dword v13, s[4:7], 0 offen lds\n"
      "buffer_load_dword v13, s[8:11], 0 offen lds\n"
      "s_waitcnt vmcnt(2)\n"
      "s_barrier\n"
      "s_branch .Lmk_helper3\n"
      ".Lmk_main:\n"
      "s_mov_b32 s14, 11534336\n"
      "s_mov_b32 s15, s7\n"
      "s_mov_b32 s19, s7\n"
      "s_mov_b32 m0, 161856\n"
      "v_and_b32 v7, 31, v1\n"
      "v_lshrrev_b32 v11, 5, v1\n"
      "s_cmp_lg_u32 s3, 3\n"
      "s_cbranch_scc1 .Lmk_nocode\n"
      "s_getpc_b64 s[16:17]\n"
      ".Lmk_pc:\n"
      "s_sub_u32 s16, s16, .Lmk_pc-.Lmk_start\n"
      "s_subb_u32 s17, s17, 0\n"
      "s_mov_b32 s18, .Lmk_end-.Lmk_start\n"
      "s_lshr_b32 s40, s2, 3\n"
      "s_lshl_b32 s40, s40, 7\n"
      "v_lshlrev_b32 v12, 12, v1\n"
      "buffer_load_dword v12, s[16:19], s40 offen lds\n"
      ".Lmk_nocode:\n"
      "s_waitcnt lgkmcnt(0)\n"
      "s_load_dwordx2 s[28:29], s[26:27], 0x0\n"
      "s_mov_b64 s[12:13], s[26:27]\n"
      "s_and_b32 s38, s3, 1\n"
      "s_lshr_b32 s39, s3, 1\n"
      "s_and_b32 s40, s2, 7\n"
      "s_lshl1_add_u32 s43, s40, s30\n"
      "s_mov_b32 s80, 0\n"
      "s_mov_b32 s81, -1\n"
      "s_mov_b32 s82, -1\n"
      "s_mov_b32 s83, 0\n"
      "v_lshlrev_b32 v8, 4, v1\n"
      "v_bfe_u32 v12, v1, 1, 3\n"
      "v_xor_b32 v12, v12, v11\n"
      "v_lshlrev_b32 v12, 4, v12\n"
      "v_lshl_or_b32 v12, v7, 7, v12\n"
      "s_lshl_b32 s40, s38, 13\n"
      "v_add_u32 v2, s40, v12\n"
      "v_and_b32 v12, 12, v1\n"
      "v_lshl_or_b32 v12, v11, 1, v12\n"
      "v_mul_u32_u24 v12, 0x150, v12\n"
      "v_and_b32 v13, 16, v1\n"
      "v_lshl_add_u32 v12, v13, 1, v12\n"
      "v_and_b32 v13, 3, v1\n"
      "v_lshl_add_u32 v12, v13, 3, v12\n"
      "s_cmp_eq_u32 s30, 0\n"
      "s_cselect_b32 s40, 168, 56\n"
      "s_mul_i32 s40, s40, s39\n"
      "s_add_u32 s40, s40, 18432\n"
      "v_add_u32_e64 v5, s40, v12\n"
      "s_mov_b32 exec_lo, 0x88880000\n"
      "s_mov_b32 exec_hi, 0x88880000\n"
      "v_add_u32 v5, 16777216, v5\n"
      "s_mov_b64 exec, -1\n"
      "s_lshl2_add_u32 s40, s43, s3\n"
      "s_lshl_b32 s40, s40, 16\n"
      "s_add_u32 s40, s40, 3145728\n"
      "v_add_u32 v10, s40, v8\n"
      "s_lshl_b32 s40, s43, 4\n"
      "s_add_u32 s40, s40, s31\n"
      "s_lshl2_add_u32 s40, s40, s3\n"
      "s_lshl_b32 s40, s40, 11\n"
      "s_add_u32 s85, s40, 1048576\n"
      "s_mov_b32 s84, 0\n"
      "s_lshl_b32 s40, s3, 11\n"
      "s_add_u32 s86, s40, 139264\n"
      "s_lshl_b32 s40, s3, 2\n"
      "s_add_u32 s93, s40, 161824\n"
      "s_mul_i32 s79, s3, 12288\n"
      "s_add_u32 s40, s3, 1\n"
      "s_and_b32 s40, s40, 3\n"
      "s_lshl_b32 s41, s40, 11\n"
      "s_add_u32 s87, s41, 139264\n"
      "s_mul_i32 s40, s40, 12288\n"
      "s_add_u32 s90, s40, 0\n"
      "s_add_u32 s40, s3, 2\n"
      "s_and_b32 s40, s40, 3\n"
      "s_lshl_b32 s41, s40, 11\n"
      "s_add_u32 s88, s41, 139264\n"
      "s_mul_i32 s40, s40, 12288\n"
      "s_add_u32 s91, s40, 4096\n"
      "s_add_u32 s40, s3, 3\n"
      "s_and_b32 s40, s40, 3\n"
      "s_lshl_b32 s41, s40, 11\n"
      "s_add_u32 s89, s41, 139264\n"
      "s_mul_i32 s40, s40, 12288\n"
      "s_add_u32 s92, s40, 8192\n"
      "v_mov_b32 v152, 0\n"
      "v_mov_b32 v153, 0\n"
      "v_mov_b32 v154, 0\n"
      "v_mov_b32 v155, 0\n"
      "v_mov_b32 v156, 0\n"
      "v_mov_b32 v157, 0\n"
      "v_mov_b32 v158, 0\n"
      "v_mov_b32 v159, 0\n"
      "v_mov_b32 v160, 0\n"
      "v_mov_b32 v161, 0\n"
      "v_mov_b32 v162, 0\n"
      "v_mov_b32 v163, 0\n"
      "v_mov_b32 v164, 0\n"
      "v_mov_b32 v165, 0\n"
      "v_mov_b32 v166, 0\n"
      "v_mov_b32 v167, 0\n"
      "v_mov_b32 v168, 0\n"
      "v_mov_b32 v169, 0\n"
      "v_mov_b32 v170, 0\n"
      "v_mov_b32 v171, 0\n"
      "v_mov_b32 v172, 0\n"
      "v_mov_b32 v173, 0\n"
      "v_mov_b32 v174, 0\n"
      "v_mov_b32_e64 v175, 0\n"
      "v_mfma_f32_32x32x16_bf16 v[16:31], v[152:155], v[152:155], 0\n"
      "v_mfma_f32_32x32x16_bf16 v[32:47], v[152:155], v[152:155], 0\n"
      "v_mfma_f32_32x32x16_bf16 v[48:63], v[152:155], v[152:155], 0\n"
      "v_mfma_f32_32x32x16_bf16 v[64:79], v[152:155], v[152:155], 0\n"
      "v_mfma_f32_32x32x16_bf16 v[80:95], v[152:155], v[152:155], 0\n"
      "v_mfma_f32_32x32x16_bf16 v[96:111], v[152:155], v[152:155], 0\n"
      "v_mfma_f32_32x32x16_bf16 v[112:127], v[152:155], v[152:155], 0\n"
      "s_mov_b32 s47, 0\n"
      "s_mov_b32 s52, 6\n"
      "s_cmp_lg_u32 s30, 0\n"
      "s_cbranch_scc1 .Lmk_setA\n"
      "s_mul_i32 s42, s34, 6\n"
      "s_mul_i32 s40, s39, 3\n"
      "s_add_u32 s42, s42, s40\n"
      "v_cmp_eq_u32 vcc, 28, v7\n"
      "v_mov_b32 v13, 1065369472\n"
      "v_and_b32 v14, 0x33, v1\n"
      "v_cndmask_b32 v12, 0, v13, vcc\n"
      "v_and_b32 v15, 4, v1\n"
      "v_lshl_or_b32 v14, v15, 1, v14\n"
      "v_and_b32 v15, 8, v1\n"
      "v_lshrrev_b32 v15, 1, v15\n"
      "v_or_b32 v14, v14, v15\n"
      "v_lshlrev_b32 v14, 4, v14\n"
      "s_lshl1_add_u32 s44, s33, s38\n"
      "s_lshl_b32 s44, s44, 12\n"
      "s_add_u32 s44, s44, 1048576\n"
      "s_lshr_b32 s45, s33, 2\n"
      "s_lshl2_add_u32 s45, s45, s38\n"
      "s_and_b32 s46, s33, 3\n"
      "s_lshl2_add_u32 s45, s45, s46\n"
      "s_lshl_b32 s45, s45, 12\n"
      "s_add_u32 s45, s45, 3145728\n"
      "v_mov_b32 v176, v12\n"
      "v_mov_b32 v177, v12\n"
      "v_mov_b32 v178, v12\n"
      "v_mov_b32 v179, v12\n"
      "v_mov_b32 v180, v12\n"
      "v_mov_b32 v181, v12\n"
      "v_mov_b32 v182, v12\n"
      "v_mov_b32 v183, v12\n"
      "s_add_u32 s40, s42, 0\n"
      "s_mov_b32 s76, s40\n"
      "s_and_b32 s41, s40, 15\n"
      "s_lshl_b32 s46, s41, 18\n"
      "s_add_u32 s56, s45, s46\n"
      "s_mov_b32 s60, 32768\n"
      "s_cmp_ge_u32 s40, 32\n"
      "s_cbranch_scc1 .Lmk_sv2\n"
      "s_mov_b64 s[64:65], -1\n"
      "s_mov_b64 s[66:67], s[82:83]\n"
      "s_cmp_ge_u32 s40, 16\n"
      "s_cbranch_scc1 .Lmk_sk1\n"
      "s_lshl_b32 s46, s41, 17\n"
      "s_add_u32 s56, s44, s46\n"
      "s_movk_i32 s60, 2048\n"
      "v_mov_b32 v182, 0\n"
      "v_mov_b32 v183, 0\n"
      "v_mov_b32 v200, v8\n"
      "s_branch .Lmk_se3\n"
      ".Lmk_sk1:\n"
      "v_mov_b32 v182, v13\n"
      "v_mov_b32 v183, 0x3f80\n"
      "v_mov_b32 v200, v14\n"
      "s_branch .Lmk_se3\n"
      ".Lmk_sv2:\n"
      "s_mov_b32 s64, 0x0fffffff\n"
      "s_mov_b32 s65, s64\n"
      "s_mov_b64 s[66:67], s[64:65]\n"
      "v_add_u32 v200, 0x800, v8\n"
      ".Lmk_se3:\n"
      "v_mov_b32 v184, v12\n"
      "v_mov_b32 v185, v12\n"
      "v_mov_b32 v186, v12\n"
      "v_mov_b32 v187, v12\n"
      "v_mov_b32 v188, v12\n"
      "v_mov_b32 v189, v12\n"
      "v_mov_b32 v190, v12\n"
      "v_mov_b32 v191, v12\n"
      "s_add_u32 s40, s42, 1\n"
      "s_mov_b32 s77, s40\n"
      "s_and_b32 s41, s40, 15\n"
      "s_lshl_b32 s46, s41, 18\n"
      "s_add_u32 s57, s45, s46\n"
      "s_mov_b32 s61, 32768\n"
      "s_cmp_ge_u32 s40, 32\n"
      "s_cbranch_scc1 .Lmk_sv5\n"
      "s_mov_b64 s[68:69], -1\n"
      "s_mov_b64 s[70:71], s[82:83]\n"
      "s_cmp_ge_u32 s40, 16\n"
      "s_cbranch_scc1 .Lmk_sk4\n"
      "s_lshl_b32 s46, s41, 17\n"
      "s_add_u32 s57, s44, s46\n"
      "s_movk_i32 s61, 2048\n"
      "v_mov_b32 v190, 0\n"
      "v_mov_b32 v191, 0\n"
      "v_mov_b32 v201, v8\n"
      "s_branch .Lmk_se6\n"
      ".Lmk_sk4:\n"
      "v_mov_b32 v190, v13\n"
      "v_mov_b32 v191, 0x3f80\n"
      "v_mov_b32 v201, v14\n"
      "s_branch .Lmk_se6\n"
      ".Lmk_sv5:\n"
      "s_mov_b32 s68, 0x0fffffff\n"
      "s_mov_b32 s69, s68\n"
      "s_mov_b64 s[70:71], s[68:69]\n"
      "v_add_u32 v201, 0x800, v8\n"
      ".Lmk_se6:\n"
      "v_mov_b32 v192, v12\n"
      "v_mov_b32 v193, v12\n"
      "v_mov_b32 v194, v12\n"
      "v_mov_b32 v195, v12\n"
      "v_mov_b32 v196, v12\n"
      "v_mov_b32 v197, v12\n"
      "v_mov_b32 v198, v12\n"
      "v_mov_b32 v199, v12\n"
      "s_add_u32 s40, s42, 2\n"
      "s_mov_b32 s78, s40\n"
      "s_and_b32 s41, s40, 15\n"
      "s_lshl_b32 s46, s41, 18\n"
      "s_add_u32 s58, s45, s46\n"
      "s_mov_b32 s62, 32768\n"
      "s_cmp_ge_u32 s40, 32\n"
      "s_cbranch_scc1 .Lmk_sv8\n"
      "s_mov_b64 s[72:73], -1\n"
      "s_mov_b64 s[74:75], s[82:83]\n"
      "s_cmp_ge_u32 s40, 16\n"
      "s_cbranch_scc1 .Lmk_sk7\n"
      "s_lshl_b32 s46, s41, 17\n"
      "s_add_u32 s58, s44, s46\n"
      "s_movk_i32 s62, 2048\n"
      "v_mov_b32 v198, 0\n"
      "v_mov_b32 v199, 0\n"
      "v_mov_b32 v202, v8\n"
      "s_branch .Lmk_se9\n"
      ".Lmk_sk7:\n"
      "v_mov_b32 v198, v13\n"
      "v_mov_b32 v199, 0x3f80\n"
      "v_mov_b32 v202, v14\n"
      "s_branch .Lmk_se9\n"
      ".Lmk_sv8:\n"
      "s_mov_b32 s72, 0x0fffffff\n"
      "s_mov_b32 s73, s72\n"
      "s_mov_b64 s[74:75], s[72:73]\n"
      "v_add_u32 v202, 0x800, v8\n"
      ".Lmk_se9:\n"
      "v_and_b32 v12, 3, v1\n"
      "v_add_u32 v13, s42, v12\n"
      "s_lshl1_add_u32 s40, s33, s38\n"
      "v_lshl_add_u32 v13, v13, 5, s40\n"
      "v_bfe_u32 v14, v1, 2, 1\n"
      "v_mul_u32_u24 v14, 196608, v14\n"
      "v_lshl_add_u32 v13, v13, 7, v14\n"
      "v_add_u32 v13, 4096, v13\n"
      "s_movk_i32 s40, 0x77\n"
      "s_mov_b32 s41, 0\n"
      "v_mov_b32 v14, 2147483647\n"
      "v_cndmask_b32 v9, v14, v13, s[40:41]\n"
      "s_cmp_lt_u32 s42, 30\n"
      "s_cbranch_scc1 .Lmk_loop0\n"
      "s_cmp_eq_u32 s42, 30\n"
      "s_cbranch_scc1 .Lmk_loop1\n"
      "s_branch .Lmk_loop2\n"
      ".Lmk_loop1:\n"
      "v_add_u32 v3, s47, v2\n"
      "v_add_u32_e64 v6, s47, v5\n"
      "s_waitcnt lgkmcnt(0)\n"
      "s_barrier\n"
      "v_mfma_f32_32x32x16_bf16 v[16:31], v[164:167], v[152:155], v[16:31]\n"
      "ds_read_b128 v[128:131], v3 offset:0\n"
      "ds_read_b64_tr_b16 v[140:141], v6 offset:0\n"
      "v_mfma_f32_32x32x16_bf16 v[32:47], v[164:167], v[156:159], v[32:47]\n"
      "ds_read_b64_tr_b16 v[142:143], v6 offset:336\n"
      "ds_read_b128 v[132:135], v3 offset:4096\n"
      "v_mfma_f32_32x32x16_bf16 v[48:63], v[168:171], v[152:155], v[48:63]\n"
      "ds_read_b64_tr_b16 v[144:145], v6 offset:56\n"
      "ds_read_b64_tr_b16 v[146:147], v6 offset:392\n"
      "v_mfma_f32_32x32x16_bf16 v[64:79], v[168:171], v[156:159], v[64:79]\n"
      "ds_read_b64_tr_b16 v[148:149], v6 offset:112\n"
      "ds_read_b64_tr_b16 v[150:151], v6 offset:448\n"
      "v_mfma_f32_32x32x16_bf16 v[80:95], v[152:155], v[172:175], v[80:95]\n"
      "v_mfma_f32_32x32x16_bf16 v[96:111], v[156:159], v[172:175], v[96:111]\n"
      "v_xor_b32 v4, 32, v3\n"
      "s_waitcnt lgkmcnt(5)\n"
      "v_mfma_f32_32x32x16_bf16 v[16:31], v[140:143], v[128:131], v[16:31]\n"
      "ds_read_b128 v[152:155], v4 offset:0\n"
      "ds_read_b64_tr_b16 v[164:165], v6 offset:5376\n"
      "s_waitcnt lgkmcnt(6)\n"
      "v_mfma_f32_32x32x16_bf16 v[32:47], v[140:143], v[132:135], v[32:47]\n"
      "ds_read_b64_tr_b16 v[166:167], v6 offset:5712\n"
      "ds_read_b128 v[156:159], v4 offset:4096\n"
      "s_waitcnt lgkmcnt(6)\n"
      "v_mfma_f32_32x32x16_bf16 v[48:63], v[144:147], v[128:131], v[48:63]\n"
      "ds_read_b64_tr_b16 v[168:169], v6 offset:5432\n"
      "ds_read_b64_tr_b16 v[170:171], v6 offset:5768\n"
      "v_mfma_f32_32x32x16_bf16 v[64:79], v[144:147], v[132:135], v[64:79]\n"
      "ds_read_b64_tr_b16 v[172:173], v6 offset:5488\n"
      "ds_read_b64_tr_b16 v[174:175], v6 offset:5824\n"
      "s_waitcnt lgkmcnt(8)\n"
      "v_mfma_f32_32x32x16_bf16 v[80:95], v[128:131], v[148:151], v[80:95]\n"
      "v_mfma_f32_32x32x16_bf16 v[96:111], v[132:135], v[148:151], v[96:111]\n"
      "v_xor_b32_e64 v4, 64, v3\n"
      "s_waitcnt lgkmcnt(5)\n"
      "v_mfma_f32_32x32x16_bf16 v[16:31], v[164:167], v[152:155], v[16:31]\n"
      "ds_read_b128 v[128:131], v4 offset:0\n"
      "ds_read_b64_tr_b16 v[140:141], v6 offset:10752\n"
      "s_waitcnt lgkmcnt(6)\n"
      "v_mfma_f32_32x32x16_bf16 v[32:47], v[164:167], v[156:159], v[32:47]\n"
      "ds_read_b64_tr_b16 v[142:143], v6 offset:11088\n"
      "ds_read_b128 v[132:135], v4 offset:4096\n"
      "s_waitcnt lgkmcnt(6)\n"
      "v_mfma_f32_32x32x16_bf16 v[48:63], v[168:171], v[152:155], v[48:63]\n"
      "ds_read_b64_tr_b16 v[144:145], v6 offset:10808\n"
      "ds_read_b64_tr_b16 v[146:147], v6 offset:11144\n"
      "v_mfma_f32_32x32x16_bf16 v[64:79], v[168:171], v[156:159], v[64:79]\n"
      "ds_read_b64_tr_b16 v[148:149], v6 offset:10864\n"
      "ds_read_b64_tr_b16 v[150:151], v6 offset:11200\n"
      "s_waitcnt lgkmcnt(8)\n"
      "v_mfma_f32_32x32x16_bf16 v[80:95], v[152:155], v[172:175], v[80:95]\n"
      "v_mfma_f32_32x32x16_bf16 v[96:111], v[156:159], v[172:175], v[96:111]\n"
      "v_xor_b32 v4, 96, v3\n"
      "s_waitcnt lgkmcnt(5)\n"
      "v_mfma_f32_32x32x16_bf16 v[16:31], v[140:143], v[128:131], v[16:31]\n"
      "ds_read_b128 v[152:155], v4 offset:0\n"
      "ds_read_b64_tr_b16 v[164:165], v6 offset:16128\n"
      "s_waitcnt lgkmcnt(6)\n"
      "v_mfma_f32_32x32x16_bf16 v[32:47], v[140:143], v[132:135], v[32:47]\n"
      "ds_read_b64_tr_b16 v[166:167], v6 offset:16464\n"
      "ds_read_b128 v[156:159], v4 offset:4096\n"
      "s_waitcnt lgkmcnt(6)\n"
      "v_mfma_f32_32x32x16_bf16 v[48:63], v[144:147], v[128:131], v[48:63]\n"
      "ds_read_b64_tr_b16 v[168:169], v6 offset:16184\n"
      "ds_read_b64_tr_b16 v[170:171], v6 offset:16520\n"
      "v_mfma_f32_32x32x16_bf16 v[64:79], v[144:147], v[132:135], v[64:79]\n"
      "ds_read_b64_tr_b16 v[172:173], v6 offset:16240\n"
      "ds_read_b64_tr_b16 v[174:175], v6 offset:16576\n"
      "s_waitcnt lgkmcnt(8)\n"
      "v_mfma_f32_32x32x16_bf16 v[80:95], v[128:131], v[148:151], v[80:95]\n"
      "s_add_u32 s47, s47, 39936\n"
      "s_cmp_eq_u32 s47, 119808\n"
      "s_cselect_b32 s47, 0, s47\n"
      "s_sub_u32 s52, s52, 1\n"
      "v_mfma_f32_32x32x16_bf16 v[96:111], v[132:135], v[148:151], v[96:111]\n"
      "s_cbranch_scc0 .Lmk_loop1\n"
      "s_waitcnt lgkmcnt(0)\n"
      "s_nop 0\n"
      "v_mfma_f32_32x32x16_bf16 v[16:31], v[164:167], v[152:155], v[16:31]\n"
      "v_mfma_f32_32x32x16_bf16 v[48:63], v[168:171], v[152:155], v[48:63]\n"
      "v_mfma_f32_32x32x16_bf16 v[80:95], v[152:155], v[172:175], v[80:95]\n"
      "v_mfma_f32_32x32x16_bf16 v[32:47], v[164:167], v[156:159], v[32:47]\n"
      "v_mfma_f32_32x32x16_bf16 v[64:79], v[168:171], v[156:159], v[64:79]\n"
      "v_mfma_f32_32x32x16_bf16 v[96:111], v[156:159], v[172:175], v[96:111]\n"
      "s_branch .Lmk_epiB\n"
      ".Lmk_loop2:\n"
      "v_add_u32 v3, s47, v2\n"
      "v_add_u32_e64 v6, s47, v5\n"
      "s_waitcnt lgkmcnt(0)\n"
      "s_barrier\n"
      "v_mfma_f32_32x32x16_bf16 v[16:31], v[152:155], v[164:167], v[16:31]\n"
      "ds_read_b128 v[128:131], v3 offset:0\n"
      "ds_read_b64_tr_b16 v[140:141], v6 offset:0\n"
      "v_mfma_f32_32x32x16_bf16 v[32:47], v[156:159], v[164:167], v[32:47]\n"
      "ds_read_b64_tr_b16 v[142:143], v6 offset:336\n"
      "ds_read_b128 v[132:135], v3 offset:4096\n"
      "v_mfma_f32_32x32x16_bf16 v[48:63], v[152:155], v[168:171], v[48:63]\n"
      "ds_read_b64_tr_b16 v[144:145], v6 offset:56\n"
      "ds_read_b64_tr_b16 v[146:147], v6 offset:392\n"
      "v_mfma_f32_32x32x16_bf16 v[64:79], v[156:159], v[168:171], v[64:79]\n"
      "ds_read_b64_tr_b16 v[148:149], v6 offset:112\n"
      "ds_read_b64_tr_b16 v[150:151], v6 offset:448\n"
      "v_mfma_f32_32x32x16_bf16 v[80:95], v[152:155], v[172:175], v[80:95]\n"
      "v_mfma_f32_32x32x16_bf16 v[96:111], v[156:159], v[172:175], v[96:111]\n"
      "v_xor_b32 v4, 32, v3\n"
      "s_waitcnt lgkmcnt(5)\n"
      "v_mfma_f32_32x32x16_bf16 v[16:31], v[128:131], v[140:143], v[16:31]\n"
      "ds_read_b128 v[152:155], v4 offset:0\n"
      "ds_read_b64_tr_b16 v[164:165], v6 offset:5376\n"
      "s_waitcnt lgkmcnt(6)\n"
      "v_mfma_f32_32x32x16_bf16 v[32:47], v[132:135], v[140:143], v[32:47]\n"
      "ds_read_b64_tr_b16 v[166:167], v6 offset:5712\n"
      "ds_read_b128 v[156:159], v4 offset:4096\n"
      "s_waitcnt lgkmcnt(6)\n"
      "v_mfma_f32_32x32x16_bf16 v[48:63], v[128:131], v[144:147], v[48:63]\n"
      "ds_read_b64_tr_b16 v[168:169], v6 offset:5432\n"
      "ds_read_b64_tr_b16 v[170:171], v6 offset:5768\n"
      "v_mfma_f32_32x32x16_bf16 v[64:79], v[132:135], v[144:147], v[64:79]\n"
      "ds_read_b64_tr_b16 v[172:173], v6 offset:5488\n"
      "ds_read_b64_tr_b16 v[174:175], v6 offset:5824\n"
      "s_waitcnt lgkmcnt(8)\n"
      "v_mfma_f32_32x32x16_bf16 v[80:95], v[128:131], v[148:151], v[80:95]\n"
      "v_mfma_f32_32x32x16_bf16 v[96:111], v[132:135], v[148:151], v[96:111]\n"
      "v_xor_b32_e64 v4, 64, v3\n"
      "s_waitcnt lgkmcnt(5)\n"
      "v_mfma_f32_32x32x16_bf16 v[16:31], v[152:155], v[164:167], v[16:31]\n"
      "ds_read_b128 v[128:131], v4 offset:0\n"
      "ds_read_b64_tr_b16 v[140:141], v6 offset:10752\n"
      "s_waitcnt lgkmcnt(6)\n"
      "v_mfma_f32_32x32x16_bf16 v[32:47], v[156:159], v[164:167], v[32:47]\n"
      "ds_read_b64_tr_b16 v[142:143], v6 offset:11088\n"
      "ds_read_b128 v[132:135], v4 offset:4096\n"
      "s_waitcnt lgkmcnt(6)\n"
      "v_mfma_f32_32x32x16_bf16 v[48:63], v[152:155], v[168:171], v[48:63]\n"
      "ds_read_b64_tr_b16 v[144:145], v6 offset:10808\n"
      "ds_read_b64_tr_b16 v[146:147], v6 offset:11144\n"
      "v_mfma_f32_32x32x16_bf16 v[64:79], v[156:159], v[168:171], v[64:79]\n"
      "ds_read_b64_tr_b16 v[148:149], v6 offset:10864\n"
      "ds_read_b64_tr_b16 v[150:151], v6 offset:11200\n"
      "s_waitcnt lgkmcnt(8)\n"
      "v_mfma_f32_32x32x16_bf16 v[80:95], v[152:155], v[172:175], v[80:95]\n"
      "v_mfma_f32_32x32x16_bf16 v[96:111], v[156:159], v[172:175], v[96:111]\n"
      "v_xor_b32 v4, 96, v3\n"
      "s_waitcnt lgkmcnt(5)\n"
      "v_mfma_f32_32x32x16_bf16 v[16:31], v[128:131], v[140:143], v[16:31]\n"
      "ds_read_b128 v[152:155], v4 offset:0\n"
      "ds_read_b64_tr_b16 v[164:165], v6 offset:16128\n"
      "s_waitcnt lgkmcnt(6)\n"
      "v_mfma_f32_32x32x16_bf16 v[32:47], v[132:135], v[140:143], v[32:47]\n"
      "ds_read_b64_tr_b16 v[166:167], v6 offset:16464\n"
      "ds_read_b128 v[156:159], v4 offset:4096\n"
      "s_waitcnt lgkmcnt(6)\n"
      "v_mfma_f32_32x32x16_bf16 v[48:63], v[128:131], v[144:147], v[48:63]\n"
      "ds_read_b64_tr_b16 v[168:169], v6 offset:16184\n"
      "ds_read_b64_tr_b16 v[170:171], v6 offset:16520\n"
      "v_mfma_f32_32x32x16_bf16 v[64:79], v[132:135], v[144:147], v[64:79]\n"
      "ds_read_b64_tr_b16 v[172:173], v6 offset:16240\n"
      "ds_read_b64_tr_b16 v[174:175], v6 offset:16576\n"
      "s_waitcnt lgkmcnt(8)\n"
      "v_mfma_f32_32x32x16_bf16 v[80:95], v[128:131], v[148:151], v[80:95]\n"
      "s_add_u32 s47, s47, 39936\n"
      "s_cmp_eq_u32 s47, 119808\n"
      "s_cselect_b32 s47, 0, s47\n"
      "s_sub_u32 s52, s52, 1\n"
      "v_mfma_f32_32x32x16_bf16 v[96:111], v[132:135], v[148:151], v[96:111]\n"
      "s_cbranch_scc0 .Lmk_loop2\n"
      "s_waitcnt lgkmcnt(0)\n"
      "s_nop 0\n"
      "v_mfma_f32_32x32x16_bf16 v[16:31], v[152:155], v[164:167], v[16:31]\n"
      "v_mfma_f32_32x32x16_bf16 v[48:63], v[152:155], v[168:171], v[48:63]\n"
      "v_mfma_f32_32x32x16_bf16 v[80:95], v[152:155], v[172:175], v[80:95]\n"
      "v_mfma_f32_32x32x16_bf16 v[32:47], v[156:159], v[164:167], v[32:47]\n"
      "v_mfma_f32_32x32x16_bf16 v[64:79], v[156:159], v[168:171], v[64:79]\n"
      "v_mfma_f32_32x32x16_bf16 v[96:111], v[156:159], v[172:175], v[96:111]\n"
      "s_branch .Lmk_epiB\n"
      ".Lmk_loop0:\n"
      "v_add_u32 v3, s47, v2\n"
      "v_add_u32_e64 v6, s47, v5\n"
      "s_waitcnt lgkmcnt(0)\n"
      "s_barrier\n"
      "v_mfma_f32_32x32x16_bf16 v[16:31], v[164:167], v[152:155], v[16:31]\n"
      "ds_read_b128 v[128:131], v3 offset:0\n"
      "ds_read_b64_tr_b16 v[140:141], v6 offset:0\n"
      "v_mfma_f32_32x32x16_bf16 v[32:47], v[164:167], v[156:159], v[32:47]\n"
      "ds_read_b64_tr_b16 v[142:143], v6 offset:336\n"
      "ds_read_b128 v[132:135], v3 offset:4096\n"
      "v_mfma_f32_32x32x16_bf16 v[48:63], v[168:171], v[152:155], v[48:63]\n"
      "ds_read_b64_tr_b16 v[144:145], v6 offset:56\n"
      "ds_read_b64_tr_b16 v[146:147], v6 offset:392\n"
      "v_mfma_f32_32x32x16_bf16 v[64:79], v[168:171], v[156:159], v[64:79]\n"
      "ds_read_b64_tr_b16 v[148:149], v6 offset:112\n"
      "ds_read_b64_tr_b16 v[150:151], v6 offset:448\n"
      "v_mfma_f32_32x32x16_bf16 v[80:95], v[172:175], v[152:155], v[80:95]\n"
      "v_mfma_f32_32x32x16_bf16 v[96:111], v[172:175], v[156:159], v[96:111]\n"
      "v_xor_b32 v4, 32, v3\n"
      "s_waitcnt lgkmcnt(5)\n"
      "v_mfma_f32_32x32x16_bf16 v[16:31], v[140:143], v[128:131], v[16:31]\n"
      "ds_read_b128 v[152:155], v4 offset:0\n"
      "ds_read_b64_tr_b16 v[164:165], v6 offset:5376\n"
      "s_waitcnt lgkmcnt(6)\n"
      "v_mfma_f32_32x32x16_bf16 v[32:47], v[140:143], v[132:135], v[32:47]\n"
      "ds_read_b64_tr_b16 v[166:167], v6 offset:5712\n"
      "ds_read_b128 v[156:159], v4 offset:4096\n"
      "s_waitcnt lgkmcnt(6)\n"
      "v_mfma_f32_32x32x16_bf16 v[48:63], v[144:147], v[128:131], v[48:63]\n"
      "ds_read_b64_tr_b16 v[168:169], v6 offset:5432\n"
      "ds_read_b64_tr_b16 v[170:171], v6 offset:5768\n"
      "v_mfma_f32_32x32x16_bf16 v[64:79], v[144:147], v[132:135], v[64:79]\n"
      "ds_read_b64_tr_b16 v[172:173], v6 offset:5488\n"
      "ds_read_b64_tr_b16 v[174:175], v6 offset:5824\n"
      "s_waitcnt lgkmcnt(8)\n"
      "v_mfma_f32_32x32x16_bf16 v[80:95], v[148:151], v[128:131], v[80:95]\n"
      "v_mfma_f32_32x32x16_bf16 v[96:111], v[148:151], v[132:135], v[96:111]\n"
      "v_xor_b32_e64 v4, 64, v3\n"
      "s_waitcnt lgkmcnt(5)\n"
      "v_mfma_f32_32x32x16_bf16 v[16:31], v[164:167], v[152:155], v[16:31]\n"
      "ds_read_b128 v[128:131], v4 offset:0\n"
      "ds_read_b64_tr_b16 v[140:141], v6 offset:10752\n"
      "s_waitcnt lgkmcnt(6)\n"
      "v_mfma_f32_32x32x16_bf16 v[32:47], v[164:167], v[156:159], v[32:47]\n"
      "ds_read_b64_tr_b16 v[142:143], v6 offset:11088\n"
      "ds_read_b128 v[132:135], v4 offset:4096\n"
      "s_waitcnt lgkmcnt(6)\n"
      "v_mfma_f32_32x32x16_bf16 v[48:63], v[168:171], v[152:155], v[48:63]\n"
      "ds_read_b64_tr_b16 v[144:145], v6 offset:10808\n"
      "ds_read_b64_tr_b16 v[146:147], v6 offset:11144\n"
      "v_mfma_f32_32x32x16_bf16 v[64:79], v[168:171], v[156:159], v[64:79]\n"
      "ds_read_b64_tr_b16 v[148:149], v6 offset:10864\n"
      "ds_read_b64_tr_b16 v[150:151], v6 offset:11200\n"
      "s_waitcnt lgkmcnt(8)\n"
      "v_mfma_f32_32x32x16_bf16 v[80:95], v[172:175], v[152:155], v[80:95]\n"
      "v_mfma_f32_32x32x16_bf16 v[96:111], v[172:175], v[156:159], v[96:111]\n"
      "v_xor_b32 v4, 96, v3\n"
      "s_waitcnt lgkmcnt(5)\n"
      "v_mfma_f32_32x32x16_bf16 v[16:31], v[140:143], v[128:131], v[16:31]\n"
      "ds_read_b128 v[152:155], v4 offset:0\n"
      "ds_read_b64_tr_b16 v[164:165], v6 offset:16128\n"
      "s_waitcnt lgkmcnt(6)\n"
      "v_mfma_f32_32x32x16_bf16 v[32:47], v[140:143], v[132:135], v[32:47]\n"
      "ds_read_b64_tr_b16 v[166:167], v6 offset:16464\n"
      "ds_read_b128 v[156:159], v4 offset:4096\n"
      "s_waitcnt lgkmcnt(6)\n"
      "v_mfma_f32_32x32x16_bf16 v[48:63], v[144:147], v[128:131], v[48:63]\n"
      "ds_read_b64_tr_b16 v[168:169], v6 offset:16184\n"
      "ds_read_b64_tr_b16 v[170:171], v6 offset:16520\n"
      "v_mfma_f32_32x32x16_bf16 v[64:79], v[144:147], v[132:135], v[64:79]\n"
      "ds_read_b64_tr_b16 v[172:173], v6 offset:16240\n"
      "ds_read_b64_tr_b16 v[174:175], v6 offset:16576\n"
      "s_waitcnt lgkmcnt(8)\n"
      "v_mfma_f32_32x32x16_bf16 v[80:95], v[148:151], v[128:131], v[80:95]\n"
      "s_add_u32 s47, s47, 39936\n"
      "s_cmp_eq_u32 s47, 119808\n"
      "s_cselect_b32 s47, 0, s47\n"
      "s_sub_u32 s52, s52, 1\n"
      "v_mfma_f32_32x32x16_bf16 v[96:111], v[148:151], v[132:135], v[96:111]\n"
      "s_cbranch_scc0 .Lmk_loop0\n"
      "s_waitcnt lgkmcnt(0)\n"
      "s_nop 0\n"
      "v_mfma_f32_32x32x16_bf16 v[16:31], v[164:167], v[152:155], v[16:31]\n"
      "v_mfma_f32_32x32x16_bf16 v[48:63], v[168:171], v[152:155], v[48:63]\n"
      "v_mfma_f32_32x32x16_bf16 v[80:95], v[172:175], v[152:155], v[80:95]\n"
      "v_mfma_f32_32x32x16_bf16 v[32:47], v[164:167], v[156:159], v[32:47]\n"
      "v_mfma_f32_32x32x16_bf16 v[64:79], v[168:171], v[156:159], v[64:79]\n"
      "v_mfma_f32_32x32x16_bf16 v[96:111], v[172:175], v[156:159], v[96:111]\n"
      ".Lmk_epiB:\n"
      "s_add_u32 s28, s28, 0x7F4A7C15\n"
      "s_addc_u32 s29, s29, 0x9E3779B9\n"
      "s_mov_b32 s40, 1049335798\n"
      "s_mov_b32 s41, s40\n"
      "v_mov_b32 v12, s28\n"
      "v_mov_b32 v13, s29\n"
      "s_cmp_ge_u32 s42, 16\n"
      "s_cbranch_scc1 .Lmk_nomul10\n"
      "v_mul_f32 v16, s40, v16\n"
      "v_mul_f32 v17, s40, v17\n"
      "v_mul_f32 v18, s40, v18\n"
      "v_mul_f32 v19, s40, v19\n"
      "v_mul_f32 v20, s40, v20\n"
      "v_mul_f32 v21, s40, v21\n"
      "v_mul_f32 v22, s40, v22\n"
      "v_mul_f32 v23, s40, v23\n"
      "v_mul_f32 v24, s40, v24\n"
      "v_mul_f32 v25, s40, v25\n"
      "v_mul_f32 v26, s40, v26\n"
      "v_mul_f32 v27, s40, v27\n"
      "v_mul_f32 v28, s40, v28\n"
      "v_mul_f32 v29, s40, v29\n"
      "v_mul_f32 v30, s40, v30\n"
      "v_mul_f32 v31, s40, v31\n"
      ".Lmk_nomul10:\n"
      "s_mov_b64 exec, s[64:65]\n"
      "v_cvt_pk_bf16_f32 v176, v16, v17\n"
      "v_cvt_pk_bf16_f32 v177, v18, v19\n"
      "v_cvt_pk_bf16_f32 v178, v20, v21\n"
      "v_cvt_pk_bf16_f32 v179, v22, v23\n"
      "v_cvt_pk_bf16_f32 v180, v24, v25\n"
      "v_cvt_pk_bf16_f32 v181, v26, v27\n"
      "s_mov_b64 exec, s[66:67]\n"
      "v_cvt_pk_bf16_f32 v182, v28, v29\n"
      "v_cvt_pk_bf16_f32 v183, v30, v31\n"
      "s_mov_b64 exec, -1\n"
      "buffer_store_dwordx4 v[176:179], v200, s[12:15], s56 offen sc1\n"
      "buffer_store_dwordx4 v[180:183], v200, s[12:15], s56 offen offset:1024 sc1\n"
      "s_add_u32 s56, s56, s60\n"
      "s_cmp_ge_u32 s42, 15\n"
      "s_cbranch_scc1 .Lmk_nomul11\n"
      "s_nop 0\n"
      "v_pk_mul_f32 v[48:49], s[40:41], v[48:49]\n"
      "v_pk_mul_f32 v[50:51], s[40:41], v[50:51]\n"
      "v_pk_mul_f32 v[52:53], s[40:41], v[52:53]\n"
      "v_pk_mul_f32 v[54:55], s[40:41], v[54:55]\n"
      "v_pk_mul_f32 v[56:57], s[40:41], v[56:57]\n"
      "v_pk_mul_f32 v[58:59], s[40:41], v[58:59]\n"
      "v_pk_mul_f32 v[60:61], s[40:41], v[60:61]\n"
      "v_pk_mul_f32 v[62:63], s[40:41], v[62:63]\n"
      ".Lmk_nomul11:\n"
      "s_mov_b64 exec, s[68:69]\n"
      "s_nop 0\n"
      "v_cvt_pk_bf16_f32 v184, v48, v49\n"
      "v_cvt_pk_bf16_f32 v185, v50, v51\n"
      "v_cvt_pk_bf16_f32 v186, v52, v53\n"
      "v_cvt_pk_bf16_f32 v187, v54, v55\n"
      "v_cvt_pk_bf16_f32 v188, v56, v57\n"
      "v_cvt_pk_bf16_f32 v189, v58, v59\n"
      "s_mov_b64 exec, s[70:71]\n"
      "v_cvt_pk_bf16_f32 v190, v60, v61\n"
      "v_cvt_pk_bf16_f32 v191, v62, v63\n"
      "s_mov_b64 exec, -1\n"
      "buffer_store_dwordx4 v[184:187], v201, s[12:15], s57 offen sc1\n"
      "buffer_store_dwordx4 v[188:191], v201, s[12:15], s57 offen offset:1024 sc1\n"
      "s_add_u32 s57, s57, s61\n"
      "s_cmp_ge_u32 s42, 14\n"
      "s_cbranch_scc1 .Lmk_nomul12\n"
      "s_nop 0\n"
      "v_pk_mul_f32 v[80:81], s[40:41], v[80:81]\n"
      "v_pk_mul_f32 v[82:83], s[40:41], v[82:83]\n"
      "v_pk_mul_f32 v[84:85], s[40:41], v[84:85]\n"
      "v_pk_mul_f32 v[86:87], s[40:41], v[86:87]\n"
      "v_pk_mul_f32 v[88:89], s[40:41], v[88:89]\n"
      "v_pk_mul_f32 v[90:91], s[40:41], v[90:91]\n"
      "v_pk_mul_f32 v[92:93], s[40:41], v[92:93]\n"
      "v_pk_mul_f32 v[94:95], s[40:41], v[94:95]\n"
      ".Lmk_nomul12:\n"
      "s_mov_b64 exec, s[72:73]\n"
      "s_nop 0\n"
      "v_cvt_pk_bf16_f32 v192, v80, v81\n"
      "v_cvt_pk_bf16_f32 v193, v82, v83\n"
      "v_cvt_pk_bf16_f32 v194, v84, v85\n"
      "v_cvt_pk_bf16_f32 v195, v86, v87\n"
      "v_cvt_pk_bf16_f32 v196, v88, v89\n"
      "v_cvt_pk_bf16_f32 v197, v90, v91\n"
      "s_mov_b64 exec, s[74:75]\n"
      "v_cvt_pk_bf16_f32 v198, v92, v93\n"
      "v_cvt_pk_bf16_f32 v199, v94, v95\n"
      "s_mov_b64 exec, -1\n"
      "buffer_store_dwordx4 v[192:195], v202, s[12:15], s58 offen sc1\n"
      "buffer_store_dwordx4 v[196:199], v202, s[12:15], s58 offen offset:1024 sc1\n"
      "s_add_u32 s58, s58, s62\n"
      "s_cmp_ge_u32 s42, 16\n"
      "s_cbranch_scc1 .Lmk_nomul13\n"
      "s_nop 0\n"
      "v_pk_mul_f32 v[32:33], s[40:41], v[32:33]\n"
      "v_pk_mul_f32 v[34:35], s[40:41], v[34:35]\n"
      "v_pk_mul_f32 v[36:37], s[40:41], v[36:37]\n"
      "v_pk_mul_f32 v[38:39], s[40:41], v[38:39]\n"
      "v_pk_mul_f32 v[40:41], s[40:41], v[40:41]\n"
      "v_pk_mul_f32 v[42:43], s[40:41], v[42:43]\n"
      "v_pk_mul_f32 v[44:45], s[40:41], v[44:45]\n"
      "v_pk_mul_f32 v[46:47], s[40:41], v[46:47]\n"
      ".Lmk_nomul13:\n"
      "s_mov_b64 exec, s[64:65]\n"
      "s_nop 0\n"
      "v_cvt_pk_bf16_f32 v176, v32, v33\n"
      "v_cvt_pk_bf16_f32 v177, v34, v35\n"
      "v_cvt_pk_bf16_f32 v178, v36, v37\n"
      "v_cvt_pk_bf16_f32 v179, v38, v39\n"
      "v_cvt_pk_bf16_f32 v180, v40, v41\n"
      "v_cvt_pk_bf16_f32 v181, v42, v43\n"
      "s_mov_b64 exec, s[66:67]\n"
      "v_cvt_pk_bf16_f32 v182, v44, v45\n"
      "v_cvt_pk_bf16_f32 v183, v46, v47\n"
      "s_mov_b64 exec, -1\n"
      "s_cmp_lt_u32 s42, 16\n"
      "s_cbranch_scc1 .Lmk_hasq14\n"
      "s_waitcnt vmcnt(0)\n"
      "s_mov_b64 exec, 7\n"
      "buffer_store_dwordx2 v[12:13], v9, s[12:15], 0 offen sc1\n"
      "v_mov_b32 v9, 2147483647\n"
      "s_mov_b64 exec, -1\n"
      "s_sleep 10\n"
      ".Lmk_hasq14:\n"
      "buffer_store_dwordx4 v[176:179], v200, s[12:15], s56 offen sc1\n"
      "buffer_store_dwordx4 v[180:183], v200, s[12:15], s56 offen offset:1024 sc1\n"
      "s_cmp_ge_u32 s42, 15\n"
      "s_cbranch_scc1 .Lmk_nomul15\n"
      "v_pk_mul_f32 v[64:65], s[40:41], v[64:65]\n"
      "v_pk_mul_f32 v[66:67], s[40:41], v[66:67]\n"
      "v_pk_mul_f32 v[68:69], s[40:41], v[68:69]\n"
      "v_pk_mul_f32 v[70:71], s[40:41], v[70:71]\n"
      "v_pk_mul_f32 v[72:73], s[40:41], v[72:73]\n"
      "v_pk_mul_f32 v[74:75], s[40:41], v[74:75]\n"
      "v_pk_mul_f32 v[76:77], s[40:41], v[76:77]\n"
      "v_pk_mul_f32 v[78:79], s[40:41], v[78:79]\n"
      ".Lmk_nomul15:\n"
      "s_mov_b64 exec, s[68:69]\n"
      "s_nop 0\n"
      "v_cvt_pk_bf16_f32 v184, v64, v65\n"
      "v_cvt_pk_bf16_f32 v185, v66, v67\n"
      "v_cvt_pk_bf16_f32 v186, v68, v69\n"
      "v_cvt_pk_bf16_f32 v187, v70, v71\n"
      "v_cvt_pk_bf16_f32 v188, v72, v73\n"
      "v_cvt_pk_bf16_f32 v189, v74, v75\n"
      "s_mov_b64 exec, s[70:71]\n"
      "v_cvt_pk_bf16_f32 v190, v76, v77\n"
      "v_cvt_pk_bf16_f32 v191, v78, v79\n"
      "s_mov_b64 exec, -1\n"
      "buffer_store_dwordx4 v[184:187], v201, s[12:15], s57 offen sc1\n"
      "buffer_store_dwordx4 v[188:191], v201, s[12:15], s57 offen offset:1024 sc1\n"
      "s_cmp_ge_u32 s42, 14\n"
      "s_cbranch_scc1 .Lmk_nomul16\n"
      "v_pk_mul_f32 v[96:97], s[40:41], v[96:97]\n"
      "v_pk_mul_f32 v[98:99], s[40:41], v[98:99]\n"
      "v_pk_mul_f32 v[100:101], s[40:41], v[100:101]\n"
      "v_pk_mul_f32 v[102:103], s[40:41], v[102:103]\n"
      "v_pk_mul_f32 v[104:105], s[40:41], v[104:105]\n"
      "v_pk_mul_f32 v[106:107], s[40:41], v[106:107]\n"
      "v_pk_mul_f32 v[108:109], s[40:41], v[108:109]\n"
      "v_pk_mul_f32 v[110:111], s[40:41], v[110:111]\n"
      ".Lmk_nomul16:\n"
      "s_mov_b64 exec, s[72:73]\n"
      "s_nop 0\n"
      "v_cvt_pk_bf16_f32 v192, v96, v97\n"
      "v_cvt_pk_bf16_f32 v193, v98, v99\n"
      "v_cvt_pk_bf16_f32 v194, v100, v101\n"
      "v_cvt_pk_bf16_f32 v195, v102, v103\n"
      "v_cvt_pk_bf16_f32 v196, v104, v105\n"
      "v_cvt_pk_bf16_f32 v197, v106, v107\n"
      "s_mov_b64 exec, s[74:75]\n"
      "v_cvt_pk_bf16_f32 v198, v108, v109\n"
      "v_cvt_pk_bf16_f32 v199, v110, v111\n"
      "s_mov_b64 exec, -1\n"
      "buffer_store_dwordx4 v[192:195], v202, s[12:15], s58 offen sc1\n"
      "buffer_store_dwordx4 v[196:199], v202, s[12:15], s58 offen offset:1024 sc1\n"
      ".Lmk_attn:\n"
      "s_waitcnt vmcnt(0)\n"
      "buffer_store_dwordx2 v[12:13], v9, s[12:15], 0 offen sc1\n"
      "v_mov_b32 v9, 2147483647\n"
      "v_mov_b32 v240, 2147483647\n"
      "s_mov_b64 s[64:65], 0\n"
      "s_barrier\n"
      "s_nop 0\n"
      "buffer_load_dwordx4 v[40:43], v8, s[12:15], s85 offen offset:0 sc1\n"
      "buffer_load_dwordx4 v[160:163], v10, s[12:15], s84 offen offset:0 sc1\n"
      "buffer_load_dwordx4 v[44:47], v8, s[12:15], s85 offen offset:1024 sc1\n"
      "buffer_load_dwordx4 v[164:167], v10, s[12:15], s84 offen offset:1024 sc1\n"
      "s_mov_b32 s84, 4096\n"
      "buffer_load_dwordx4 v[176:179], v10, s[12:15], s84 offen offset:0 sc1\n"
      "buffer_load_dwordx4 v[180:183], v10, s[12:15], s84 offen offset:1024 sc1\n"
      "s_mov_b32 s84, 0\n"
      "s_nop 0\n"
      "buffer_load_dwordx4 v[168:171], v10, s[12:15], s84 offen offset:2048 sc1\n"
      "buffer_load_dwordx4 v[172:175], v10, s[12:15], s84 offen offset:3072 sc1\n"
      "s_mov_b32 s84, 4096\n"
      "buffer_load_dwordx4 v[184:187], v10, s[12:15], s84 offen offset:2048 sc1\n"
      "buffer_load_dwordx4 v[188:191], v10, s[12:15], s84 offen offset:3072 sc1\n"
      "s_mov_b32 s84, 8192\n"
      "buffer_load_dwordx4 v[192:195], v10, s[12:15], s84 offen offset:0 sc1\n"
      "buffer_load_dwordx4 v[196:199], v10, s[12:15], s84 offen offset:1024 sc1\n"
      "buffer_load_dwordx4 v[200:203], v10, s[12:15], s84 offen offset:2048 sc1\n"
      "buffer_load_dwordx4 v[204:207], v10, s[12:15], s84 offen offset:3072 sc1\n"
      ".Lmk_pad0:\n"
      ".Lmk_attn2:\n"
      "s_cbranch_execz .Lmk_padr0\n"
      "v_mov_b32 v212, 0\n"
      "v_mov_b32 v213, 0\n"
      "v_mov_b32 v214, 0\n"
      "v_mov_b32 v215, 0\n"
      "v_add_u32 v11, s86, v8\n"
      "v_mov_b32 v3, 0xdddd\n"
      "s_waitcnt vmcnt(12)\n"
      "v_mfma_f32_32x32x16_bf16 v[112:127], v[160:163], v[40:43], 0\n"
      "ds_write_b128 v11, v[40:43]\n"
      "s_waitcnt vmcnt(10)\n"
      "v_mfma_f32_32x32x16_bf16 v[144:159], v[164:167], v[44:47], v[112:127]\n"
      "s_waitcnt vmcnt(9)\n"
      "v_mfma_f32_32x32x16_bf16 v[128:143], v[176:179], v[40:43], 0\n"
      "s_waitcnt vmcnt(8)\n"
      "v_mfma_f32_32x32x16_bf16 v[48:63], v[180:183], v[44:47], v[128:143]\n"
      "v_mfma_f32_32x32x16_bf16 v[96:111], v[212:215], v[212:215], 0\n"
      "v_add_u32 v208, s87, v8\n"
      "v_add_u32 v4, s90, v8\n"
      "v_add_u32 v209, s88, v8\n"
      "v_add_u32 v5, s91, v8\n"
      "v_add_u32 v210, s89, v8\n"
      "v_add_u32 v6, s92, v8\n"
      "v_mov_b32 v14, s93\n"
      "v_add_u32 v2, s79, v8\n"
      "v_max3_f32 v144, v144, v145, v146\n"
      "v_max3_f32 v147, v147, v148, v149\n"
      "v_max3_f32 v144, v144, v150, v151\n"
      "v_max3_f32 v147, v147, v152, v153\n"
      "v_max3_f32 v144, v144, v154, v155\n"
      "v_max3_f32 v147, v147, v156, v157\n"
      "v_max3_f32 v144, v144, v158, v159\n"
      "v_max3_f32 v147, v147, v48, v49\n"
      "v_max3_f32 v144, v144, v50, v51\n"
      "v_max3_f32 v147, v147, v52, v53\n"
      "v_max3_f32 v144, v144, v54, v55\n"
      "v_max3_f32 v147, v147, v56, v57\n"
      "v_max3_f32 v144, v144, v58, v59\n"
      "v_max3_f32 v147, v147, v60, v61\n"
      "v_max3_f32 v144, v144, v62, v63\n"
      "v_max_f32 v144, v144, v147\n"
      "v_mov_b32 v145, v144\n"
      "s_nop 1\n"
      "v_permlane32_swap_b32 v144, v145\n"
      "v_max_f32_e64 v144, v144, v145\n"
      "v_sub_f32 v144, 0xc2c40000, v144\n"
      "s_mov_b64 exec, s[80:81]\n"
      "v_cvt_pk_bf16_f32 v46, v144, 0\n"
      "s_mov_b64 exec, -1\n"
      "ds_write_b128 v11, v[44:47] offset:1024\n"
      "v_mfma_f32_32x32x16_bf16 v[112:127], v[164:167], v[44:47], v[112:127]\n"
      "v_mfma_f32_32x32x16_bf16 v[48:63], v[212:215], v[212:215], 0\n"
      "s_nop 10\n"
      "v_exp_f32 v112, v112\n"
      "v_exp_f32 v113, v113\n"
      "v_exp_f32 v114, v114\n"
      "v_exp_f32 v115, v115\n"
      "v_mfma_f32_32x32x16_bf16 v[128:143], v[180:183], v[44:47], v[128:143]\n"
      "s_waitcnt vmcnt(7)\n"
      "v_exp_f32 v116, v116\n"
      "v_exp_f32 v117, v117\n"
      "v_exp_f32 v118, v118\n"
      "v_exp_f32 v119, v119\n"
      "v_mfma_f32_32x32x16_bf16 v[64:79], v[212:215], v[212:215], 0\n"
      "s_waitcnt lgkmcnt(0)\n"
      "s_barrier\n"
      "v_mfma_f32_32x32x16_bf16 v[80:95], v[212:215], v[212:215], 0\n"
      "ds_read_b128 v[16:19], v208 offset:0\n"
      "ds_read_b128 v[20:23], v208 offset:1024\n"
      "ds_read_b128 v[24:27], v209 offset:0\n"
      "ds_read_b128 v[28:31], v209 offset:1024\n"
      "ds_read_b128 v[32:35], v210 offset:0\n"
      "ds_read_b128 v[36:39], v210 offset:1024\n"
      "v_exp_f32 v120, v120\n"
      "v_exp_f32 v121, v121\n"
      "v_exp_f32 v122, v122\n"
      "v_exp_f32 v123, v123\n"
      "v_smfmac_f32_32x32x32_bf16 v[96:111], v[168:171], v[112:119], v3\n"
      "v_exp_f32 v124, v124\n"
      "v_exp_f32 v125, v125\n"
      "v_exp_f32 v126, v126\n"
      "v_exp_f32 v127, v127\n"
      "s_waitcnt lgkmcnt(5)\n"
      "v_mfma_f32_32x32x16_bf16 v[144:159], v[160:163], v[16:19], 0\n"
      "v_exp_f32 v128, v128\n"
      "v_exp_f32 v129, v129\n"
      "v_exp_f32 v130, v130\n"
      "v_exp_f32 v131, v131\n"
      "s_waitcnt lgkmcnt(4)\n"
      "v_mfma_f32_32x32x16_bf16 v[144:159], v[164:167], v[20:23], v[144:159]\n"
      "buffer_store_dwordx4 v[224:227], v240, s[12:15], s64 offen offset:0 sc1\n"
      "s_waitcnt vmcnt(7)\n"
      "v_exp_f32 v132, v132\n"
      "v_exp_f32 v133, v133\n"
      "v_exp_f32 v134, v134\n"
      "v_exp_f32 v135, v135\n"
      "v_smfmac_f32_32x32x32_bf16 v[96:111], v[172:175], v[120:127], v3\n"
      "s_waitcnt vmcnt(6)\n"
      "v_exp_f32 v136, v136\n"
      "v_exp_f32 v137, v137\n"
      "v_exp_f32 v138, v138\n"
      "v_exp_f32 v139, v139\n"
      "v_smfmac_f32_32x32x32_bf16 v[96:111], v[184:187], v[128:135], v3\n"
      "v_exp_f32 v140, v140\n"
      "v_exp_f32 v141, v141\n"
      "v_exp_f32 v142, v142\n"
      "v_exp_f32 v143, v143\n"
      "s_waitcnt lgkmcnt(0)\n"
      "v_mfma_f32_32x32x16_bf16 v[112:127], v[160:163], v[24:27], 0\n"
      "v_exp_f32 v144, v144\n"
      "v_exp_f32 v145, v145\n"
      "v_exp_f32 v146, v146\n"
      "v_exp_f32 v147, v147\n"
      "v_mfma_f32_32x32x16_bf16 v[112:127], v[164:167], v[28:31], v[112:127]\n"
      "buffer_store_dwordx4 v[228:231], v240, s[12:15], s64 offen offset:1024 sc1\n"
      "s_waitcnt vmcnt(6)\n"
      "v_exp_f32 v148, v148\n"
      "v_exp_f32 v149, v149\n"
      "v_exp_f32 v150, v150\n"
      "v_exp_f32 v151, v151\n"
      "v_smfmac_f32_32x32x32_bf16 v[96:111], v[188:191], v[136:143], v3\n"
      "v_exp_f32 v152, v152\n"
      "v_exp_f32 v153, v153\n"
      "v_exp_f32 v154, v154\n"
      "v_exp_f32 v155, v155\n"
      "v_smfmac_f32_32x32x32_bf16 v[48:63], v[168:171], v[144:151], v3\n"
      "v_exp_f32 v156, v156\n"
      "v_exp_f32 v157, v157\n"
      "v_exp_f32 v158, v158\n"
      "v_exp_f32 v159, v159\n"
      "v_mfma_f32_32x32x16_bf16 v[128:143], v[160:163], v[32:35], 0\n"
      "s_mov_b32 s84, 12288\n"
      "buffer_load_dwordx4 v[160:163], v10, s[12:15], s84 offen offset:0 sc1\n"
      "v_exp_f32 v112, v112\n"
      "v_exp_f32 v113, v113\n"
      "v_exp_f32 v114, v114\n"
      "v_exp_f32 v115, v115\n"
      "v_mfma_f32_32x32x16_bf16 v[128:143], v[164:167], v[36:39], v[128:143]\n"
      "buffer_store_dwordx4 v[232:235], v240, s[12:15], s65 offen offset:0 sc1\n"
      "v_exp_f32 v116, v116\n"
      "v_exp_f32 v117, v117\n"
      "v_exp_f32 v118, v118\n"
      "v_exp_f32 v119, v119\n"
      "v_smfmac_f32_32x32x32_bf16 v[48:63], v[172:175], v[152:159], v3\n"
      "buffer_load_dwordx4 v[164:167], v10, s[12:15], s84 offen offset:1024 sc1\n"
      "v_exp_f32 v120, v120\n"
      "v_exp_f32 v121, v121\n"
      "v_exp_f32 v122, v122\n"
      "v_exp_f32 v123, v123\n"
      "v_smfmac_f32_32x32x32_bf16 v[64:79], v[168:171], v[112:119], v3\n"
      "v_exp_f32 v124, v124\n"
      "v_exp_f32 v125, v125\n"
      "v_exp_f32 v126, v126\n"
      "v_exp_f32 v127, v127\n"
      "v_mfma_f32_32x32x16_bf16 v[144:159], v[176:179], v[16:19], 0\n"
      "v_exp_f32 v128, v128\n"
      "v_exp_f32 v129, v129\n"
      "v_exp_f32 v130, v130\n"
      "v_exp_f32 v131, v131\n"
      "v_mfma_f32_32x32x16_bf16 v[144:159], v[180:183], v[20:23], v[144:159]\n"
      "buffer_store_dwordx4 v[236:239], v240, s[12:15], s65 offen offset:1024 sc1\n"
      "v_exp_f32 v132, v132\n"
      "v_exp_f32 v133, v133\n"
      "v_exp_f32 v134, v134\n"
      "v_exp_f32 v135, v135\n"
      "v_smfmac_f32_32x32x32_bf16 v[64:79], v[172:175], v[120:127], v3\n"
      "v_exp_f32 v136, v136\n"
      "v_exp_f32 v137, v137\n"
      "v_exp_f32 v138, v138\n"
      "v_exp_f32 v139, v139\n"
      "v_smfmac_f32_32x32x32_bf16 v[80:95], v[168:171], v[128:135], v3\n"
      "v_exp_f32 v140, v140\n"
      "v_exp_f32 v141, v141\n"
      "v_exp_f32 v142, v142\n"
      "v_exp_f32 v143, v143\n"
      "v_mfma_f32_32x32x16_bf16 v[112:127], v[176:179], v[24:27], 0\n"
      "buffer_load_dwordx4 v[168:171], v10, s[12:15], s84 offen offset:2048 sc1\n"
      "v_exp_f32 v144, v144\n"
      "v_exp_f32 v145, v145\n"
      "v_exp_f32 v146, v146\n"
      "v_exp_f32 v147, v147\n"
      "v_mfma_f32_32x32x16_bf16 v[112:127], v[180:183], v[28:31], v[112:127]\n"
      "v_exp_f32 v148, v148\n"
      "v_exp_f32 v149, v149\n"
      "v_exp_f32 v150, v150\n"
      "v_exp_f32 v151, v151\n"
      "v_smfmac_f32_32x32x32_bf16 v[80:95], v[172:175], v[136:143], v3\n"
      "buffer_load_dwordx4 v[172:175], v10, s[12:15], s84 offen offset:3072 sc1\n"
      "v_exp_f32 v152, v152\n"
      "v_exp_f32 v153, v153\n"
      "v_exp_f32 v154, v154\n"
      "v_exp_f32 v155, v155\n"
      "v_smfmac_f32_32x32x32_bf16 v[48:63], v[184:187], v[144:151], v3\n"
      "v_exp_f32 v156, v156\n"
      "v_exp_f32 v157, v157\n"
      "v_exp_f32 v158, v158\n"
      "v_exp_f32 v159, v159\n"
      "v_mfma_f32_32x32x16_bf16 v[128:143], v[176:179], v[32:35], 0\n"
      "s_mov_b32 s84, 16384\n"
      "buffer_load_dwordx4 v[176:179], v10, s[12:15], s84 offen offset:0 sc1\n"
      "v_exp_f32 v112, v112\n"
      "v_exp_f32 v113, v113\n"
      "v_exp_f32 v114, v114\n"
      "v_exp_f32 v115, v115\n"
      "v_mfma_f32_32x32x16_bf16 v[128:143], v[180:183], v[36:39], v[128:143]\n"
      "v_exp_f32 v116, v116\n"
      "v_exp_f32 v117, v117\n"
      "v_exp_f32 v118, v118\n"
      "v_exp_f32 v119, v119\n"
      "v_smfmac_f32_32x32x32_bf16 v[48:63], v[188:191], v[152:159], v3\n"
      "buffer_load_dwordx4 v[180:183], v10, s[12:15], s84 offen offset:1024 sc1\n"
      "v_exp_f32 v120, v120\n"
      "v_exp_f32 v121, v121\n"
      "v_exp_f32 v122, v122\n"
      "v_exp_f32 v123, v123\n"
      "v_smfmac_f32_32x32x32_bf16 v[64:79], v[184:187], v[112:119], v3\n"
      "s_waitcnt vmcnt(13)\n"
      "v_exp_f32 v124, v124\n"
      "v_exp_f32 v125, v125\n"
      "v_exp_f32 v126, v126\n"
      "v_exp_f32 v127, v127\n"
      "v_mfma_f32_32x32x16_bf16 v[144:159], v[192:195], v[16:19], 0\n"
      "s_waitcnt vmcnt(12)\n"
      "v_exp_f32 v128, v128\n"
      "v_exp_f32 v129, v129\n"
      "v_exp_f32 v130, v130\n"
      "v_exp_f32 v131, v131\n"
      "v_mfma_f32_32x32x16_bf16 v[144:159], v[196:199], v[20:23], v[144:159]\n"
      "v_exp_f32 v132, v132\n"
      "v_exp_f32 v133, v133\n"
      "v_exp_f32 v134, v134\n"
      "v_exp_f32 v135, v135\n"
      "v_smfmac_f32_32x32x32_bf16 v[64:79], v[188:191], v[120:127], v3\n"
      "v_exp_f32 v136, v136\n"
      "v_exp_f32 v137, v137\n"
      "v_exp_f32 v138, v138\n"
      "v_exp_f32 v139, v139\n"
      "v_smfmac_f32_32x32x32_bf16 v[80:95], v[184:187], v[128:135], v3\n"
      "v_exp_f32 v140, v140\n"
      "v_exp_f32 v141, v141\n"
      "v_exp_f32 v142, v142\n"
      "v_exp_f32 v143, v143\n"
      "v_mfma_f32_32x32x16_bf16 v[112:127], v[192:195], v[24:27], 0\n"
      "buffer_load_dwordx4 v[184:187], v10, s[12:15], s84 offen offset:2048 sc1\n"
      "v_exp_f32 v144, v144\n"
      "v_exp_f32 v145, v145\n"
      "v_exp_f32 v146, v146\n"
      "v_exp_f32 v147, v147\n"
      "v_mfma_f32_32x32x16_bf16 v[112:127], v[196:199], v[28:31], v[112:127]\n"
      "v_exp_f32 v148, v148\n"
      "v_exp_f32 v149, v149\n"
      "v_exp_f32 v150, v150\n"
      "v_exp_f32 v151, v151\n"
      "v_smfmac_f32_32x32x32_bf16 v[80:95], v[188:191], v[136:143], v3\n"
      "buffer_load_dwordx4 v[188:191], v10, s[12:15], s84 offen offset:3072 sc1\n"
      "s_waitcnt vmcnt(13)\n"
      "v_exp_f32 v152, v152\n"
      "v_exp_f32 v153, v153\n"
      "v_exp_f32 v154, v154\n"
      "v_exp_f32 v155, v155\n"
      "v_smfmac_f32_32x32x32_bf16 v[48:63], v[200:203], v[144:151], v3\n"
      "v_exp_f32 v156, v156\n"
      "v_exp_f32 v157, v157\n"
      "v_exp_f32 v158, v158\n"
      "v_exp_f32 v159, v159\n"
      "v_mfma_f32_32x32x16_bf16 v[128:143], v[192:195], v[32:35], 0\n"
      "v_exp_f32 v112, v112\n"
      "v_exp_f32 v113, v113\n"
      "v_exp_f32 v114, v114\n"
      "v_exp_f32 v115, v115\n"
      "v_mfma_f32_32x32x16_bf16 v[128:143], v[196:199], v[36:39], v[128:143]\n"
      "s_waitcnt vmcnt(12)\n"
      "v_exp_f32 v116, v116\n"
      "v_exp_f32 v117, v117\n"
      "v_exp_f32 v118, v118\n"
      "v_exp_f32 v119, v119\n"
      "v_smfmac_f32_32x32x32_bf16 v[48:63], v[204:207], v[152:159], v3\n"
      "v_exp_f32 v120, v120\n"
      "v_exp_f32 v121, v121\n"
      "v_exp_f32 v122, v122\n"
      "v_exp_f32 v123, v123\n"
      "v_smfmac_f32_32x32x32_bf16 v[64:79], v[200:203], v[112:119], v3\n"
      "v_exp_f32 v124, v124\n"
      "v_exp_f32 v125, v125\n"
      "v_exp_f32 v126, v126\n"
      "v_exp_f32 v127, v127\n"
      "v_mfma_f32_32x32x16_bf16 v[144:159], v[192:195], v[40:43], 0\n"
      "s_mov_b32 s84, 20480\n"
      "buffer_load_dwordx4 v[192:195], v10, s[12:15], s84 offen offset:0 sc1\n"
      "v_exp_f32 v128, v128\n"
      "v_exp_f32 v129, v129\n"
      "v_exp_f32 v130, v130\n"
      "v_exp_f32 v131, v131\n"
      "v_mfma_f32_32x32x16_bf16 v[144:159], v[196:199], v[44:47], v[144:159]\n"
      "v_exp_f32 v132, v132\n"
      "v_exp_f32 v133, v133\n"
      "v_exp_f32 v134, v134\n"
      "v_exp_f32 v135, v135\n"
      "v_smfmac_f32_32x32x32_bf16 v[64:79], v[204:207], v[120:127], v3\n"
      "buffer_load_dwordx4 v[196:199], v10, s[12:15], s84 offen offset:1024 sc1\n"
      "v_exp_f32 v136, v136\n"
      "v_exp_f32 v137, v137\n"
      "v_exp_f32 v138, v138\n"
      "v_exp_f32 v139, v139\n"
      "v_smfmac_f32_32x32x32_bf16 v[80:95], v[200:203], v[128:135], v3\n"
      "s_waitcnt vmcnt(11)\n"
      "v_exp_f32 v140, v140\n"
      "v_exp_f32 v141, v141\n"
      "v_exp_f32 v142, v142\n"
      "v_exp_f32 v143, v143\n"
      "v_mfma_f32_32x32x16_bf16 v[112:127], v[160:163], v[16:19], 0\n"
      "s_waitcnt vmcnt(9)\n"
      "v_exp_f32 v144, v144\n"
      "v_exp_f32 v145, v145\n"
      "v_exp_f32 v146, v146\n"
      "v_exp_f32 v147, v147\n"
      "v_mfma_f32_32x32x16_bf16 v[112:127], v[164:167], v[20:23], v[112:127]\n"
      "v_exp_f32 v148, v148\n"
      "v_exp_f32 v149, v149\n"
      "v_exp_f32 v150, v150\n"
      "v_exp_f32 v151, v151\n"
      "v_smfmac_f32_32x32x32_bf16 v[80:95], v[204:207], v[136:143], v3\n"
      "v_exp_f32 v152, v152\n"
      "v_exp_f32 v153, v153\n"
      "v_exp_f32 v154, v154\n"
      "v_exp_f32 v155, v155\n"
      "v_smfmac_f32_32x32x32_bf16 v[96:111], v[200:203], v[144:151], v3\n"
      "v_exp_f32 v156, v156\n"
      "v_exp_f32 v157, v157\n"
      "v_exp_f32 v158, v158\n"
      "v_exp_f32 v159, v159\n"
      "v_mfma_f32_32x32x16_bf16 v[128:143], v[160:163], v[24:27], 0\n"
      "buffer_load_dwordx4 v[200:203], v10, s[12:15], s84 offen offset:2048 sc1\n"
      "v_exp_f32 v112, v112\n"
      "v_exp_f32 v113, v113\n"
      "v_exp_f32 v114, v114\n"
      "v_exp_f32 v115, v115\n"
      "v_mfma_f32_32x32x16_bf16 v[128:143], v[164:167], v[28:31], v[128:143]\n"
      "v_exp_f32 v116, v116\n"
      "v_exp_f32 v117, v117\n"
      "v_exp_f32 v118, v118\n"
      "v_exp_f32 v119, v119\n"
      "v_smfmac_f32_32x32x32_bf16 v[96:111], v[204:207], v[152:159], v3\n"
      "buffer_load_dwordx4 v[204:207], v10, s[12:15], s84 offen offset:3072 sc1\n"
      "s_waitcnt vmcnt(9)\n"
      "v_exp_f32 v120, v120\n"
      "v_exp_f32 v121, v121\n"
      "v_exp_f32 v122, v122\n"
      "v_exp_f32 v123, v123\n"
      "v_smfmac_f32_32x32x32_bf16 v[48:63], v[168:171], v[112:119], v3\n"
      "buffer_store_dwordx2 v[12:13], v9, s[12:15], 0 offen sc1\n"
      "v_exp_f32 v124, v124\n"
      "v_exp_f32 v125, v125\n"
      "v_exp_f32 v126, v126\n"
      "v_exp_f32 v127, v127\n"
      "v_mfma_f32_32x32x16_bf16 v[144:159], v[160:163], v[32:35], 0\n"
      "v_exp_f32 v128, v128\n"
      "v_exp_f32 v129, v129\n"
      "v_exp_f32 v130, v130\n"
      "v_exp_f32 v131, v131\n"
      "v_mfma_f32_32x32x16_bf16 v[144:159], v[164:167], v[36:39], v[144:159]\n"
      "s_waitcnt vmcnt(9)\n"
      "v_exp_f32 v132, v132\n"
      "v_exp_f32 v133, v133\n"
      "v_exp_f32 v134, v134\n"
      "v_exp_f32 v135, v135\n"
      "v_smfmac_f32_32x32x32_bf16 v[48:63], v[172:175], v[120:127], v3\n"
      "v_exp_f32 v136, v136\n"
      "v_exp_f32 v137, v137\n"
      "v_exp_f32 v138, v138\n"
      "v_exp_f32 v139, v139\n"
      "v_smfmac_f32_32x32x32_bf16 v[64:79], v[168:171], v[128:135], v3\n"
      "v_exp_f32 v140, v140\n"
      "v_exp_f32 v141, v141\n"
      "v_exp_f32 v142, v142\n"
      "v_exp_f32 v143, v143\n"
      "v_mfma_f32_32x32x16_bf16 v[112:127], v[160:163], v[40:43], 0\n"
      "s_mov_b32 s84, 24576\n"
      "buffer_load_dwordx4 v[160:163], v10, s[12:15], s84 offen offset:0 sc1\n"
      "v_exp_f32 v144, v144\n"
      "v_exp_f32 v145, v145\n"
      "v_exp_f32 v146, v146\n"
      "v_exp_f32 v147, v147\n"
      "v_mfma_f32_32x32x16_bf16 v[112:127], v[164:167], v[44:47], v[112:127]\n"
      "v_exp_f32 v148, v148\n"
      "v_exp_f32 v149, v149\n"
      "v_exp_f32 v150, v150\n"
      "v_exp_f32 v151, v151\n"
      "v_smfmac_f32_32x32x32_bf16 v[64:79], v[172:175], v[136:143], v3\n"
      "buffer_load_dwordx4 v[164:167], v10, s[12:15], s84 offen offset:1024 sc1\n"
      "v_exp_f32 v152, v152\n"
      "v_exp_f32 v153, v153\n"
      "v_exp_f32 v154, v154\n"
      "v_exp_f32 v155, v155\n"
      "v_smfmac_f32_32x32x32_bf16 v[80:95], v[168:171], v[144:151], v3\n"
      "s_waitcnt vmcnt(10)\n"
      "v_exp_f32 v156, v156\n"
      "v_exp_f32 v157, v157\n"
      "v_exp_f32 v158, v158\n"
      "v_exp_f32 v159, v159\n"
      "v_mfma_f32_32x32x16_bf16 v[128:143], v[176:179], v[16:19], 0\n"
      "s_waitcnt vmcnt(9)\n"
      "v_exp_f32 v112, v112\n"
      "v_exp_f32 v113, v113\n"
      "v_exp_f32 v114, v114\n"
      "v_exp_f32 v115, v115\n"
      "v_mfma_f32_32x32x16_bf16 v[128:143], v[180:183], v[20:23], v[128:143]\n"
      "v_exp_f32 v116, v116\n"
      "v_exp_f32 v117, v117\n"
      "v_exp_f32 v118, v118\n"
      "v_exp_f32 v119, v119\n"
      "v_smfmac_f32_32x32x32_bf16 v[80:95], v[172:175], v[152:159], v3\n"
      "v_exp_f32 v120, v120\n"
      "v_exp_f32 v121, v121\n"
      "v_exp_f32 v122, v122\n"
      "v_exp_f32 v123, v123\n"
      "v_smfmac_f32_32x32x32_bf16 v[96:111], v[168:171], v[112:119], v3\n"
      "v_exp_f32 v124, v124\n"
      "v_exp_f32 v125, v125\n"
      "v_exp_f32 v126, v126\n"
      "v_exp_f32 v127, v127\n"
      "v_mfma_f32_32x32x16_bf16 v[144:159], v[176:179], v[24:27], 0\n"
      "buffer_load_dwordx4 v[168:171], v10, s[12:15], s84 offen offset:2048 sc1\n"
      "v_exp_f32 v128, v128\n"
      "v_exp_f32 v129, v129\n"
      "v_exp_f32 v130, v130\n"
      "v_exp_f32 v131, v131\n"
      "v_mfma_f32_32x32x16_bf16 v[144:159], v[180:183], v[28:31], v[144:159]\n"
      "v_exp_f32 v132, v132\n"
      "v_exp_f32 v133, v133\n"
      "v_exp_f32 v134, v134\n"
      "v_exp_f32 v135, v135\n"
      "v_smfmac_f32_32x32x32_bf16 v[96:111], v[172:175], v[120:127], v3\n"
      "buffer_load_dwordx4 v[172:175], v10, s[12:15], s84 offen offset:3072 sc1\n"
      "s_waitcnt vmcnt(10)\n"
      "v_exp_f32 v136, v136\n"
      "v_exp_f32 v137, v137\n"
      "v_exp_f32 v138, v138\n"
      "v_exp_f32 v139, v139\n"
      "v_smfmac_f32_32x32x32_bf16 v[48:63], v[184:187], v[128:135], v3\n"
      "v_exp_f32 v140, v140\n"
      "v_exp_f32 v141, v141\n"
      "v_exp_f32 v142, v142\n"
      "v_exp_f32 v143, v143\n"
      "v_mfma_f32_32x32x16_bf16 v[112:127], v[176:179], v[32:35], 0\n"
      "v_exp_f32 v144, v144\n"
      "v_exp_f32 v145, v145\n"
      "v_exp_f32 v146, v146\n"
      "v_exp_f32 v147, v147\n"
      "v_mfma_f32_32x32x16_bf16 v[112:127], v[180:183], v[36:39], v[112:127]\n"
      "s_waitcnt vmcnt(9)\n"
      "v_exp_f32 v148, v148\n"
      "v_exp_f32 v149, v149\n"
      "v_exp_f32 v150, v150\n"
      "v_exp_f32 v151, v151\n"
      "v_smfmac_f32_32x32x32_bf16 v[48:63], v[188:191], v[136:143], v3\n"
      "v_exp_f32 v152, v152\n"
      "v_exp_f32 v153, v153\n"
      ".Lmk_pad1:\n"
      "s_cbranch_execz .Lmk_padr1\n"
      "s_nop 0\n"
      "v_exp_f32 v154, v154\n"
      "v_exp_f32 v155, v155\n"
      "v_smfmac_f32_32x32x32_bf16 v[64:79], v[184:187], v[144:151], v3\n"
      "v_exp_f32 v156, v156\n"
      "v_exp_f32 v157, v157\n"
      "v_exp_f32 v158, v158\n"
      "v_exp_f32 v159, v159\n"
      "v_mfma_f32_32x32x16_bf16 v[128:143], v[176:179], v[40:43], 0\n"
      "s_mov_b32 s84, 28672\n"
      "buffer_load_dwordx4 v[176:179], v10, s[12:15], s84 offen offset:0 sc1\n"
      "v_exp_f32 v112, v112\n"
      "v_exp_f32 v113, v113\n"
      "v_exp_f32 v114, v114\n"
      "v_exp_f32 v115, v115\n"
      "v_mfma_f32_32x32x16_bf16 v[128:143], v[180:183], v[44:47], v[128:143]\n"
      "v_exp_f32 v116, v116\n"
      "v_exp_f32 v117, v117\n"
      "v_exp_f32 v118, v118\n"
      "v_exp_f32 v119, v119\n"
      "v_smfmac_f32_32x32x32_bf16 v[64:79], v[188:191], v[152:159], v3\n"
      "buffer_load_dwordx4 v[180:183], v10, s[12:15], s84 offen offset:1024 sc1\n"
      "v_exp_f32 v120, v120\n"
      "v_exp_f32 v121, v121\n"
      "v_exp_f32 v122, v122\n"
      "v_exp_f32 v123, v123\n"
      "v_smfmac_f32_32x32x32_bf16 v[80:95], v[184:187], v[112:119], v3\n"
      "s_waitcnt vmcnt(10)\n"
      "v_exp_f32 v124, v124\n"
      "v_exp_f32 v125, v125\n"
      "v_exp_f32 v126, v126\n"
      "v_exp_f32 v127, v127\n"
      "v_mfma_f32_32x32x16_bf16 v[144:159], v[192:195], v[16:19], 0\n"
      "s_waitcnt vmcnt(9)\n"
      "v_exp_f32 v128, v128\n"
      "v_exp_f32 v129, v129\n"
      "v_exp_f32 v130, v130\n"
      "v_exp_f32 v131, v131\n"
      "v_mfma_f32_32x32x16_bf16 v[144:159], v[196:199], v[20:23], v[144:159]\n"
      "v_exp_f32 v132, v132\n"
      "v_exp_f32 v133, v133\n"
      "v_exp_f32 v134, v134\n"
      "v_exp_f32 v135, v135\n"
      "v_smfmac_f32_32x32x32_bf16 v[80:95], v[188:191], v[120:127], v3\n"
      "v_exp_f32 v136, v136\n"
      "v_exp_f32 v137, v137\n"
      "v_exp_f32 v138, v138\n"
      "v_exp_f32 v139, v139\n"
      "v_smfmac_f32_32x32x32_bf16 v[96:111], v[184:187], v[128:135], v3\n"
      "v_exp_f32 v140, v140\n"
      "v_exp_f32 v141, v141\n"
      "v_exp_f32 v142, v142\n"
      "v_exp_f32 v143, v143\n"
      "v_mfma_f32_32x32x16_bf16 v[112:127], v[192:195], v[24:27], 0\n"
      "buffer_load_dwordx4 v[184:187], v10, s[12:15], s84 offen offset:2048 sc1\n"
      "v_exp_f32 v144, v144\n"
      "v_exp_f32 v145, v145\n"
      "v_exp_f32 v146, v146\n"
      "v_exp_f32 v147, v147\n"
      "v_mfma_f32_32x32x16_bf16 v[112:127], v[196:199], v[28:31], v[112:127]\n"
      "v_exp_f32 v148, v148\n"
      "v_exp_f32 v149, v149\n"
      "v_exp_f32 v150, v150\n"
      "v_exp_f32 v151, v151\n"
      "v_smfmac_f32_32x32x32_bf16 v[96:111], v[188:191], v[136:143], v3\n"
      "buffer_load_dwordx4 v[188:191], v10, s[12:15], s84 offen offset:3072 sc1\n"
      "ds_read_b32 v15, v14\n"
      "s_mov_b32 s95, 1048576\n"
      "s_waitcnt vmcnt(10)\n"
      "v_exp_f32 v152, v152\n"
      "v_exp_f32 v153, v153\n"
      "v_exp_f32 v154, v154\n"
      "v_exp_f32 v155, v155\n"
      "v_smfmac_f32_32x32x32_bf16 v[48:63], v[200:203], v[144:151], v3\n"
      "v_exp_f32 v156, v156\n"
      "v_exp_f32 v157, v157\n"
      "v_exp_f32 v158, v158\n"
      "v_exp_f32 v159, v159\n"
      "v_mfma_f32_32x32x16_bf16 v[128:143], v[192:195], v[32:35], 0\n"
      "v_exp_f32 v112, v112\n"
      "v_exp_f32 v113, v113\n"
      "v_exp_f32 v114, v114\n"
      "v_exp_f32 v115, v115\n"
      "v_mfma_f32_32x32x16_bf16 v[128:143], v[196:199], v[36:39], v[128:143]\n"
      "s_waitcnt vmcnt(9)\n"
      "v_exp_f32 v116, v116\n"
      "v_exp_f32 v117, v117\n"
      "v_exp_f32 v118, v118\n"
      "v_exp_f32 v119, v119\n"
      "v_smfmac_f32_32x32x32_bf16 v[48:63], v[204:207], v[152:159], v3\n"
      "v_exp_f32 v120, v120\n"
      "v_exp_f32 v121, v121\n"
      "v_exp_f32 v122, v122\n"
      "v_exp_f32 v123, v123\n"
      "v_smfmac_f32_32x32x32_bf16 v[64:79], v[200:203], v[112:119], v3\n"
      "v_exp_f32 v124, v124\n"
      "v_exp_f32 v125, v125\n"
      "v_exp_f32 v126, v126\n"
      "v_exp_f32 v127, v127\n"
      "v_mfma_f32_32x32x16_bf16 v[144:159], v[192:195], v[40:43], 0\n"
      ".Lmk_Lr:\n"
      "s_waitcnt lgkmcnt(0)\n"
      "v_cmp_eq_u32 vcc, 0, v15\n"
      "s_cbranch_vccnz .Lmk_Lw\n"
      ".Lmk_Lr2:\n"
      "s_mov_b32 s84, 32768\n"
      "buffer_load_dwordx4 v[192:195], v10, s[12:15], s84 offen offset:0 sc1\n"
      "v_exp_f32 v128, v128\n"
      "v_exp_f32 v129, v129\n"
      "v_exp_f32 v130, v130\n"
      "v_exp_f32 v131, v131\n"
      "v_mfma_f32_32x32x16_bf16 v[144:159], v[196:199], v[44:47], v[144:159]\n"
      "v_exp_f32 v132, v132\n"
      "v_exp_f32 v133, v133\n"
      "v_exp_f32 v134, v134\n"
      "v_exp_f32 v135, v135\n"
      "v_smfmac_f32_32x32x32_bf16 v[64:79], v[204:207], v[120:127], v3\n"
      "buffer_load_dwordx4 v[196:199], v10, s[12:15], s84 offen offset:1024 sc1\n"
      "v_exp_f32 v136, v136\n"
      "v_exp_f32 v137, v137\n"
      "v_exp_f32 v138, v138\n"
      "v_exp_f32 v139, v139\n"
      "v_smfmac_f32_32x32x32_bf16 v[80:95], v[200:203], v[128:135], v3\n"
      "s_waitcnt vmcnt(9)\n"
      "v_exp_f32 v140, v140\n"
      "v_exp_f32 v141, v141\n"
      "v_exp_f32 v142, v142\n"
      "v_exp_f32 v143, v143\n"
      "v_mfma_f32_32x32x16_bf16 v[112:127], v[160:163], v[16:19], 0\n"
      "s_waitcnt vmcnt(8)\n"
      "v_exp_f32 v144, v144\n"
      "v_exp_f32 v145, v145\n"
      "v_exp_f32 v146, v146\n"
      "v_exp_f32 v147, v147\n"
      "v_mfma_f32_32x32x16_bf16 v[112:127], v[164:167], v[20:23], v[112:127]\n"
      "v_exp_f32 v148, v148\n"
      "v_exp_f32 v149, v149\n"
      "v_exp_f32 v150, v150\n"
      "v_exp_f32 v151, v151\n"
      "v_smfmac_f32_32x32x32_bf16 v[80:95], v[204:207], v[136:143], v3\n"
      "v_exp_f32 v152, v152\n"
      "v_exp_f32 v153, v153\n"
      "v_exp_f32 v154, v154\n"
      "v_exp_f32 v155, v155\n"
      "v_smfmac_f32_32x32x32_bf16 v[96:111], v[200:203], v[144:151], v3\n"
      "v_exp_f32 v156, v156\n"
      "v_exp_f32 v157, v157\n"
      "v_exp_f32 v158, v158\n"
      "v_exp_f32 v159, v159\n"
      "v_mfma_f32_32x32x16_bf16 v[128:143], v[160:163], v[24:27], 0\n"
      "buffer_load_dwordx4 v[200:203], v10, s[12:15], s84 offen offset:2048 sc1\n"
      "v_exp_f32 v112, v112\n"
      "v_exp_f32 v113, v113\n"
      "v_exp_f32 v114, v114\n"
      "v_exp_f32 v115, v115\n"
      "v_mfma_f32_32x32x16_bf16 v[128:143], v[164:167], v[28:31], v[128:143]\n"
      "v_exp_f32 v116, v116\n"
      "v_exp_f32 v117, v117\n"
      "v_exp_f32 v118, v118\n"
      "v_exp_f32 v119, v119\n"
      "v_smfmac_f32_32x32x32_bf16 v[96:111], v[204:207], v[152:159], v3\n"
      "buffer_load_dwordx4 v[204:207], v10, s[12:15], s84 offen offset:3072 sc1\n"
      "s_waitcnt vmcnt(9)\n"
      "v_exp_f32 v120, v120\n"
      "v_exp_f32 v121, v121\n"
      "v_exp_f32 v122, v122\n"
      "v_exp_f32 v123, v123\n"
      "v_smfmac_f32_32x32x32_bf16 v[48:63], v[168:171], v[112:119], v3\n"
      "v_exp_f32 v124, v124\n"
      "v_exp_f32 v125, v125\n"
      "v_exp_f32 v126, v126\n"
      "v_exp_f32 v127, v127\n"
      "v_mfma_f32_32x32x16_bf16 v[144:159], v[160:163], v[32:35], 0\n"
      "v_exp_f32 v128, v128\n"
      "v_exp_f32 v129, v129\n"
      "v_exp_f32 v130, v130\n"
      "v_exp_f32 v131, v131\n"
      "v_mfma_f32_32x32x16_bf16 v[144:159], v[164:167], v[36:39], v[144:159]\n"
      "s_waitcnt vmcnt(8)\n"
      "v_exp_f32 v132, v132\n"
      "v_exp_f32 v133, v133\n"
      "v_exp_f32 v134, v134\n"
      "v_exp_f32 v135, v135\n"
      "v_smfmac_f32_32x32x32_bf16 v[48:63], v[172:175], v[120:127], v3\n"
      "v_exp_f32 v136, v136\n"
      "v_exp_f32 v137, v137\n"
      "v_exp_f32 v138, v138\n"
      "v_exp_f32 v139, v139\n"
      "v_smfmac_f32_32x32x32_bf16 v[64:79], v[168:171], v[128:135], v3\n"
      "v_exp_f32 v140, v140\n"
      "v_exp_f32 v141, v141\n"
      "v_exp_f32 v142, v142\n"
      "v_exp_f32_e64 v143, v143\n"
      "v_mfma_f32_32x32x16_bf16 v[112:127], v[160:163], v[40:43], 0\n"
      "s_mov_b32 s84, 36864\n"
      "buffer_load_dwordx4 v[160:163], v10, s[12:15], s84 offen offset:0 sc1\n"
      "v_exp_f32 v144, v144\n"
      "v_exp_f32 v145, v145\n"
      "v_exp_f32 v146, v146\n"
      "v_exp_f32 v147, v147\n"
      "v_mfma_f32_32x32x16_bf16 v[112:127], v[164:167], v[44:47], v[112:127]\n"
      "v_exp_f32 v148, v148\n"
      "v_exp_f32 v149, v149\n"
      "v_exp_f32 v150, v150\n"
      "v_exp_f32 v151, v151\n"
      "v_smfmac_f32_32x32x32_bf16 v[64:79], v[172:175], v[136:143], v3\n"
      "buffer_load_dwordx4 v[164:167], v10, s[12:15], s84 offen offset:1024 sc1\n"
      "v_exp_f32 v152, v152\n"
      "v_exp_f32 v153, v153\n"
      "v_exp_f32 v154, v154\n"
      "v_exp_f32 v155, v155\n"
      "v_smfmac_f32_32x32x32_bf16 v[80:95], v[168:171], v[144:151], v3\n"
      "s_waitcnt vmcnt(9)\n"
      "v_exp_f32 v156, v156\n"
      "v_exp_f32 v157, v157\n"
      "v_exp_f32 v158, v158\n"
      "v_exp_f32 v159, v159\n"
      "v_mfma_f32_32x32x16_bf16 v[128:143], v[176:179], v[16:19], 0\n"
      "s_waitcnt vmcnt(8)\n"
      "v_exp_f32 v112, v112\n"
      "v_exp_f32 v113, v113\n"
      "v_exp_f32 v114, v114\n"
      "v_exp_f32 v115, v115\n"
      "v_mfma_f32_32x32x16_bf16 v[128:143], v[180:183], v[20:23], v[128:143]\n"
      "v_exp_f32 v116, v116\n"
      "v_exp_f32 v117, v117\n"
      "v_exp_f32 v118, v118\n"
      "v_exp_f32 v119, v119\n"
      "v_smfmac_f32_32x32x32_bf16 v[80:95], v[172:175], v[152:159], v3\n"
      "v_exp_f32 v120, v120\n"
      "v_exp_f32 v121, v121\n"
      "v_exp_f32 v122, v122\n"
      "v_exp_f32 v123, v123\n"
      "v_smfmac_f32_32x32x32_bf16 v[96:111], v[168:171], v[112:119], v3\n"
      "v_exp_f32 v124, v124\n"
      "v_exp_f32 v125, v125\n"
      "v_exp_f32 v126, v126\n"
      "v_exp_f32 v127, v127\n"
      "v_mfma_f32_32x32x16_bf16 v[144:159], v[176:179], v[24:27], 0\n"
      "buffer_load_dwordx4 v[168:171], v10, s[12:15], s84 offen offset:2048 sc1\n"
      "v_exp_f32 v128, v128\n"
      "v_exp_f32 v129, v129\n"
      "v_exp_f32 v130, v130\n"
      "v_exp_f32 v131, v131\n"
      "v_mfma_f32_32x32x16_bf16 v[144:159], v[180:183], v[28:31], v[144:159]\n"
      "v_exp_f32 v132, v132\n"
      "v_exp_f32 v133, v133\n"
      "v_exp_f32 v134, v134\n"
      "v_exp_f32 v135, v135\n"
      "v_smfmac_f32_32x32x32_bf16 v[96:111], v[172:175], v[120:127], v3\n"
      "buffer_load_dwordx4 v[172:175], v10, s[12:15], s84 offen offset:3072 sc1\n"
      "s_waitcnt vmcnt(9)\n"
      "v_exp_f32 v136, v136\n"
      "v_exp_f32 v137, v137\n"
      "v_exp_f32 v138, v138\n"
      "v_exp_f32 v139, v139\n"
      "v_smfmac_f32_32x32x32_bf16 v[48:63], v[184:187], v[128:135], v3\n"
      "v_exp_f32 v140, v140\n"
      "v_exp_f32 v141, v141\n"
      "v_exp_f32 v142, v142\n"
      "v_exp_f32 v143, v143\n"
      "v_mfma_f32_32x32x16_bf16 v[112:127], v[176:179], v[32:35], 0\n"
      "v_exp_f32 v144, v144\n"
      "v_exp_f32 v145, v145\n"
      "v_exp_f32 v146, v146\n"
      "v_exp_f32 v147, v147\n"
      "v_mfma_f32_32x32x16_bf16 v[112:127], v[180:183], v[36:39], v[112:127]\n"
      "s_waitcnt vmcnt(8)\n"
      "v_exp_f32 v148, v148\n"
      "v_exp_f32 v149, v149\n"
      "v_exp_f32 v150, v150\n"
      "v_exp_f32 v151, v151\n"
      "v_smfmac_f32_32x32x32_bf16 v[48:63], v[188:191], v[136:143], v3\n"
      "v_exp_f32 v152, v152\n"
      "v_exp_f32 v153, v153\n"
      "v_exp_f32 v154, v154\n"
      "v_exp_f32 v155, v155\n"
      "v_smfmac_f32_32x32x32_bf16 v[64:79], v[184:187], v[144:151], v3\n"
      "v_exp_f32 v156, v156\n"
      "v_exp_f32 v157, v157\n"
      "v_exp_f32 v158, v158\n"
      "v_exp_f32 v159, v159\n"
      "v_mfma_f32_32x32x16_bf16 v[128:143], v[176:179], v[40:43], 0\n"
      "s_mov_b32 s84, 40960\n"
      "buffer_load_dwordx4 v[176:179], v10, s[12:15], s84 offen offset:0 sc1\n"
      "v_exp_f32 v112, v112\n"
      "v_exp_f32 v113, v113\n"
      "v_exp_f32 v114, v114\n"
      "v_exp_f32 v115, v115\n"
      "v_mfma_f32_32x32x16_bf16 v[128:143], v[180:183], v[44:47], v[128:143]\n"
      "v_exp_f32 v116, v116\n"
      "v_exp_f32 v117, v117\n"
      "v_exp_f32 v118, v118\n"
      "v_exp_f32 v119, v119\n"
      "v_smfmac_f32_32x32x32_bf16 v[64:79], v[188:191], v[152:159], v3\n"
      "buffer_load_dwordx4 v[180:183], v10, s[12:15], s84 offen offset:1024 sc1\n"
      "v_exp_f32 v120, v120\n"
      "v_exp_f32 v121, v121\n"
      "v_exp_f32 v122, v122\n"
      "v_exp_f32 v123, v123\n"
      "v_smfmac_f32_32x32x32_bf16 v[80:95], v[184:187], v[112:119], v3\n"
      "s_waitcnt vmcnt(9)\n"
      "v_exp_f32 v124, v124\n"
      "v_exp_f32 v125, v125\n"
      "v_exp_f32 v126, v126\n"
      "v_exp_f32 v127, v127\n"
      "v_mfma_f32_32x32x16_bf16 v[144:159], v[192:195], v[16:19], 0\n"
      "s_waitcnt vmcnt(8)\n"
      "v_exp_f32 v128, v128\n"
      "v_exp_f32 v129, v129\n"
      "v_exp_f32 v130, v130\n"
      "v_exp_f32 v131, v131\n"
      "v_mfma_f32_32x32x16_bf16 v[144:159], v[196:199], v[20:23], v[144:159]\n"
      "v_exp_f32 v132, v132\n"
      "v_exp_f32 v133, v133\n"
      "v_exp_f32 v134, v134\n"
      "v_exp_f32 v135, v135\n"
      "v_smfmac_f32_32x32x32_bf16 v[80:95], v[188:191], v[120:127], v3\n"
      "v_exp_f32 v136, v136\n"
      "v_exp_f32 v137, v137\n"
      "v_exp_f32 v138, v138\n"
      "v_exp_f32 v139, v139\n"
      "v_smfmac_f32_32x32x32_bf16 v[96:111], v[184:187], v[128:135], v3\n"
      "v_exp_f32 v140, v140\n"
      "v_exp_f32 v141, v141\n"
      "v_exp_f32 v142, v142\n"
      "v_exp_f32 v143, v143\n"
      "v_mfma_f32_32x32x16_bf16 v[112:127], v[192:195], v[24:27], 0\n"
      "buffer_load_dwordx4 v[184:187], v10, s[12:15], s84 offen offset:2048 sc1\n"
      "v_exp_f32 v144, v144\n"
      "v_exp_f32 v145, v145\n"
      "v_exp_f32 v146, v146\n"
      "v_exp_f32 v147, v147\n"
      "v_mfma_f32_32x32x16_bf16 v[112:127], v[196:199], v[28:31], v[112:127]\n"
      "v_exp_f32 v148, v148\n"
      "v_exp_f32 v149, v149\n"
      "v_exp_f32 v150, v150\n"
      "v_exp_f32 v151, v151\n"
      "v_smfmac_f32_32x32x32_bf16 v[96:111], v[188:191], v[136:143], v3\n"
      "buffer_load_dwordx4 v[188:191], v10, s[12:15], s84 offen offset:3072 sc1\n"
      "s_waitcnt vmcnt(9)\n"
      "v_exp_f32 v152, v152\n"
      "v_exp_f32 v153, v153\n"
      "v_exp_f32 v154, v154\n"
      "v_exp_f32 v155, v155\n"
      "v_smfmac_f32_32x32x32_bf16 v[48:63], v[200:203], v[144:151], v3\n"
      "v_exp_f32 v156, v156\n"
      "v_exp_f32 v157, v157\n"
      "v_exp_f32 v158, v158\n"
      "v_exp_f32 v159, v159\n"
      "v_mfma_f32_32x32x16_bf16 v[128:143], v[192:195], v[32:35], 0\n"
      "v_exp_f32 v112, v112\n"
      "v_exp_f32 v113, v113\n"
      "v_exp_f32 v114, v114\n"
      "v_exp_f32 v115, v115\n"
      "v_mfma_f32_32x32x16_bf16 v[128:143], v[196:199], v[36:39], v[128:143]\n"
      "s_waitcnt vmcnt(8)\n"
      "v_exp_f32 v116, v116\n"
      "v_exp_f32 v117, v117\n"
      "v_exp_f32 v118, v118\n"
      "v_exp_f32 v119, v119\n"
      "v_smfmac_f32_32x32x32_bf16 v[48:63], v[204:207], v[152:159], v3\n"
      "v_exp_f32 v120, v120\n"
      "v_exp_f32 v121, v121\n"
      "v_exp_f32 v122, v122\n"
      "v_exp_f32 v123, v123\n"
      "v_smfmac_f32_32x32x32_bf16 v[64:79], v[200:203], v[112:119], v3\n"
      "v_exp_f32 v124, v124\n"
      "v_exp_f32 v125, v125\n"
      "v_exp_f32 v126, v126\n"
      "v_exp_f32 v127, v127\n"
      "v_mfma_f32_32x32x16_bf16 v[144:159], v[192:195], v[40:43], 0\n"
      "s_mov_b32 s84, 45056\n"
      "buffer_load_dwordx4 v[192:195], v10, s[12:15], s84 offen offset:0 sc1\n"
      "v_exp_f32 v128, v128\n"
      "v_exp_f32 v129, v129\n"
      "v_exp_f32 v130, v130\n"
      "v_exp_f32 v131, v131\n"
      "v_mfma_f32_32x32x16_bf16 v[144:159], v[196:199], v[44:47], v[144:159]\n"
      "v_exp_f32 v132, v132\n"
      "v_exp_f32 v133, v133\n"
      "v_exp_f32 v134, v134\n"
      "v_exp_f32 v135, v135\n"
      "v_smfmac_f32_32x32x32_bf16 v[64:79], v[204:207], v[120:127], v3\n"
      "buffer_load_dwordx4 v[196:199], v10, s[12:15], s84 offen offset:1024 sc1\n"
      "v_exp_f32 v136, v136\n"
      "v_exp_f32 v137, v137\n"
      "v_exp_f32 v138, v138\n"
      "v_exp_f32 v139, v139\n"
      "v_smfmac_f32_32x32x32_bf16 v[80:95], v[200:203], v[128:135], v3\n"
      "s_waitcnt vmcnt(9)\n"
      "v_exp_f32 v140, v140\n"
      "v_exp_f32 v141, v141\n"
      "v_exp_f32 v142, v142\n"
      "v_exp_f32 v143, v143\n"
      "v_mfma_f32_32x32x16_bf16 v[112:127], v[160:163], v[16:19], 0\n"
      "s_waitcnt vmcnt(8)\n"
      "v_exp_f32 v144, v144\n"
      "v_exp_f32 v145, v145\n"
      "v_exp_f32 v146, v146\n"
      "v_exp_f32 v147, v147\n"
      "v_mfma_f32_32x32x16_bf16 v[112:127], v[164:167], v[20:23], v[112:127]\n"
      "v_exp_f32 v148, v148\n"
      "v_exp_f32 v149, v149\n"
      "v_exp_f32 v150, v150\n"
      "v_exp_f32 v151, v151\n"
      "v_smfmac_f32_32x32x32_bf16 v[80:95], v[204:207], v[136:143], v3\n"
      "v_exp_f32 v152, v152\n"
      "v_exp_f32 v153, v153\n"
      "v_exp_f32 v154, v154\n"
      "v_exp_f32 v155, v155\n"
      "v_smfmac_f32_32x32x32_bf16 v[96:111], v[200:203], v[144:151], v3\n"
      "v_exp_f32 v156, v156\n"
      "v_exp_f32 v157, v157\n"
      "v_exp_f32 v158, v158\n"
      "v_exp_f32 v159, v159\n"
      "v_mfma_f32_32x32x16_bf16 v[128:143], v[160:163], v[24:27], 0\n"
      "buffer_load_dwordx4 v[200:203], v10, s[12:15], s84 offen offset:2048 sc1\n"
      "v_exp_f32 v112, v112\n"
      "v_exp_f32 v113, v113\n"
      "v_exp_f32 v114, v114\n"
      "v_exp_f32 v115, v115\n"
      "v_mfma_f32_32x32x16_bf16 v[128:143], v[164:167], v[28:31], v[128:143]\n"
      "v_exp_f32 v116, v116\n"
      "v_exp_f32 v117, v117\n"
      "v_exp_f32 v118, v118\n"
      "v_exp_f32 v119, v119\n"
      "v_smfmac_f32_32x32x32_bf16 v[96:111], v[204:207], v[152:159], v3\n"
      "buffer_load_dwordx4 v[204:207], v10, s[12:15], s84 offen offset:3072 sc1\n"
      "s_waitcnt vmcnt(9)\n"
      "v_exp_f32 v120, v120\n"
      "v_exp_f32 v121, v121\n"
      "v_exp_f32 v122, v122\n"
      "v_exp_f32 v123, v123\n"
      "v_smfmac_f32_32x32x32_bf16 v[48:63], v[168:171], v[112:119], v3\n"
      "v_exp_f32 v124, v124\n"
      "v_exp_f32 v125, v125\n"
      "v_exp_f32 v126, v126\n"
      "v_exp_f32 v127, v127\n"
      "v_mfma_f32_32x32x16_bf16 v[144:159], v[160:163], v[32:35], 0\n"
      "v_exp_f32 v128, v128\n"
      "v_exp_f32 v129, v129\n"
      "v_exp_f32 v130, v130\n"
      "v_exp_f32 v131, v131\n"
      "v_mfma_f32_32x32x16_bf16 v[144:159], v[164:167], v[36:39], v[144:159]\n"
      "s_waitcnt vmcnt(8)\n"
      "v_exp_f32 v132, v132\n"
      "v_exp_f32 v133, v133\n"
      "v_exp_f32 v134, v134\n"
      "v_exp_f32 v135, v135\n"
      "v_smfmac_f32_32x32x32_bf16 v[48:63], v[172:175], v[120:127], v3\n"
      "v_exp_f32 v136, v136\n"
      "v_exp_f32 v137, v137\n"
      "v_exp_f32 v138, v138\n"
      "v_exp_f32 v139, v139\n"
      "v_smfmac_f32_32x32x32_bf16 v[64:79], v[168:171], v[128:135], v3\n"
      "v_exp_f32 v140, v140\n"
      "v_exp_f32 v141, v141\n"
      "v_exp_f32 v142, v142\n"
      "v_exp_f32 v143, v143\n"
      "v_mfma_f32_32x32x16_bf16 v[112:127], v[160:163], v[40:43], 0\n"
      "s_mov_b32 s84, 49152\n"
      "buffer_load_dwordx4 v[160:163], v10, s[12:15], s84 offen offset:0 sc1\n"
      "v_exp_f32 v144, v144\n"
      "v_exp_f32 v145, v145\n"
      "v_exp_f32 v146, v146\n"
      "v_exp_f32 v147, v147\n"
      "v_mfma_f32_32x32x16_bf16 v[112:127], v[164:167], v[44:47], v[112:127]\n"
      "v_exp_f32 v148, v148\n"
      "v_exp_f32 v149, v149\n"
      "v_exp_f32 v150, v150\n"
      "v_exp_f32 v151, v151\n"
      "v_smfmac_f32_32x32x32_bf16 v[64:79], v[172:175], v[136:143], v3\n"
      "buffer_load_dwordx4 v[164:167], v10, s[12:15], s84 offen offset:1024 sc1\n"
      "v_exp_f32 v152, v152\n"
      "v_exp_f32 v153, v153\n"
      "v_exp_f32 v154, v154\n"
      "v_exp_f32 v155, v155\n"
      "v_smfmac_f32_32x32x32_bf16 v[80:95], v[168:171], v[144:151], v3\n"
      "s_waitcnt vmcnt(9)\n"
      "v_exp_f32 v156, v156\n"
      "v_exp_f32 v157, v157\n"
      "v_exp_f32 v158, v158\n"
      "v_exp_f32 v159, v159\n"
      "v_mfma_f32_32x32x16_bf16 v[128:143], v[176:179], v[16:19], 0\n"
      "s_waitcnt vmcnt(8)\n"
      "v_exp_f32 v112, v112\n"
      "v_exp_f32 v113, v113\n"
      "v_exp_f32 v114, v114\n"
      "v_exp_f32 v115, v115\n"
      "v_mfma_f32_32x32x16_bf16 v[128:143], v[180:183], v[20:23], v[128:143]\n"
      "v_exp_f32 v116, v116\n"
      "v_exp_f32 v117, v117\n"
      "v_exp_f32 v118, v118\n"
      "v_exp_f32 v119, v119\n"
      "v_smfmac_f32_32x32x32_bf16 v[80:95], v[172:175], v[152:159], v3\n"
      "v_exp_f32 v120, v120\n"
      "v_exp_f32 v121, v121\n"
      "v_exp_f32 v122, v122\n"
      "v_exp_f32 v123, v123\n"
      "v_smfmac_f32_32x32x32_bf16 v[96:111], v[168:171], v[112:119], v3\n"
      "v_exp_f32 v124, v124\n"
      "v_exp_f32 v125, v125\n"
      "v_exp_f32 v126, v126\n"
      "v_exp_f32 v127, v127\n"
      "v_mfma_f32_32x32x16_bf16 v[144:159], v[176:179], v[24:27], 0\n"
      "buffer_load_dwordx4 v[168:171], v10, s[12:15], s84 offen offset:2048 sc1\n"
      "v_exp_f32 v128, v128\n"
      "v_exp_f32 v129, v129\n"
      "v_exp_f32 v130, v130\n"
      "v_exp_f32 v131, v131\n"
      "v_mfma_f32_32x32x16_bf16 v[144:159], v[180:183], v[28:31], v[144:159]\n"
      "v_exp_f32 v132, v132\n"
      "v_exp_f32 v133, v133\n"
      "v_exp_f32 v134, v134\n"
      "v_exp_f32 v135, v135\n"
      "v_smfmac_f32_32x32x32_bf16 v[96:111], v[172:175], v[120:127], v3\n"
      "buffer_load_dwordx4 v[172:175], v10, s[12:15], s84 offen offset:3072 sc1\n"
      "s_waitcnt vmcnt(9)\n"
      "v_exp_f32 v136, v136\n"
      "v_exp_f32 v137, v137\n"
      "v_exp_f32 v138, v138\n"
      "v_exp_f32 v139, v139\n"
      "v_smfmac_f32_32x32x32_bf16 v[48:63], v[184:187], v[128:135], v3\n"
      "v_exp_f32 v140, v140\n"
      "v_exp_f32 v141, v141\n"
      "v_exp_f32 v142, v142\n"
      "v_exp_f32 v143, v143\n"
      "v_mfma_f32_32x32x16_bf16 v[112:127], v[176:179], v[32:35], 0\n"
      "v_exp_f32 v144, v144\n"
      "v_exp_f32 v145, v145\n"
      "v_exp_f32 v146, v146\n"
      "v_exp_f32 v147, v147\n"
      "v_mfma_f32_32x32x16_bf16 v[112:127], v[180:183], v[36:39], v[112:127]\n"
      "s_waitcnt vmcnt(8)\n"
      "v_exp_f32 v148, v148\n"
      "v_exp_f32 v149, v149\n"
      "v_exp_f32 v150, v150\n"
      "v_exp_f32 v151, v151\n"
      "v_smfmac_f32_32x32x32_bf16 v[48:63], v[188:191], v[136:143], v3\n"
      "v_exp_f32 v152, v152\n"
      "v_exp_f32 v153, v153\n"
      "v_exp_f32 v154, v154\n"
      "v_exp_f32 v155, v155\n"
      "v_smfmac_f32_32x32x32_bf16 v[64:79], v[184:187], v[144:151], v3\n"
      "v_exp_f32 v156, v156\n"
      "v_exp_f32 v157, v157\n"
      "v_exp_f32 v158, v158\n"
      "v_exp_f32 v159, v159\n"
      "v_mfma_f32_32x32x16_bf16 v[128:143], v[176:179], v[40:43], 0\n"
      "s_mov_b32 s84, 53248\n"
      "buffer_load_dwordx4 v[176:179], v10, s[12:15], s84 offen offset:0 sc1\n"
      "v_exp_f32 v112, v112\n"
      "v_exp_f32 v113, v113\n"
      "v_exp_f32 v114, v114\n"
      "v_exp_f32 v115, v115\n"
      "v_mfma_f32_32x32x16_bf16 v[128:143], v[180:183], v[44:47], v[128:143]\n"
      "v_exp_f32 v116, v116\n"
      "v_exp_f32 v117, v117\n"
      "v_exp_f32 v118, v118\n"
      "v_exp_f32 v119, v119\n"
      "v_smfmac_f32_32x32x32_bf16 v[64:79], v[188:191], v[152:159], v3\n"
      "buffer_load_dwordx4 v[180:183], v10, s[12:15], s84 offen offset:1024 sc1\n"
      "v_exp_f32 v120, v120\n"
      "v_exp_f32 v121, v121\n"
      "v_exp_f32 v122, v122\n"
      "v_exp_f32 v123, v123\n"
      "v_smfmac_f32_32x32x32_bf16 v[80:95], v[184:187], v[112:119], v3\n"
      "s_waitcnt vmcnt(9)\n"
      "v_exp_f32 v124, v124\n"
      "v_exp_f32 v125, v125\n"
      "v_exp_f32 v126, v126\n"
      "v_exp_f32 v127, v127\n"
      "v_mfma_f32_32x32x16_bf16 v[144:159], v[192:195], v[16:19], 0\n"
      "s_waitcnt vmcnt(8)\n"
      "v_exp_f32 v128, v128\n"
      "v_exp_f32 v129, v129\n"
      "v_exp_f32 v130, v130\n"
      "v_exp_f32 v131, v131\n"
      "v_mfma_f32_32x32x16_bf16 v[144:159], v[196:199], v[20:23], v[144:159]\n"
      "v_exp_f32 v132, v132\n"
      "v_exp_f32 v133, v133\n"
      "v_exp_f32 v134, v134\n"
      "v_exp_f32 v135, v135\n"
      "v_smfmac_f32_32x32x32_bf16 v[80:95], v[188:191], v[120:127], v3\n"
      "v_exp_f32 v136, v136\n"
      "v_exp_f32 v137, v137\n"
      "v_exp_f32 v138, v138\n"
      "v_exp_f32 v139, v139\n"
      "v_smfmac_f32_32x32x32_bf16 v[96:111], v[184:187], v[128:135], v3\n"
      "v_exp_f32 v140, v140\n"
      "v_exp_f32 v141, v141\n"
      "v_exp_f32 v142, v142\n"
      "v_exp_f32 v143, v143\n"
      "v_mfma_f32_32x32x16_bf16 v[112:127], v[192:195], v[24:27], 0\n"
      "buffer_load_dwordx4 v[184:187], v10, s[12:15], s84 offen offset:2048 sc1\n"
      "v_exp_f32 v144, v144\n"
      "v_exp_f32 v145, v145\n"
      "v_exp_f32 v146, v146\n"
      "v_exp_f32 v147, v147\n"
      "v_mfma_f32_32x32x16_bf16 v[112:127], v[196:199], v[28:31], v[112:127]\n"
      "v_exp_f32 v148, v148\n"
      "v_exp_f32 v149, v149\n"
      "v_exp_f32 v150, v150\n"
      "v_exp_f32 v151, v151\n"
      "v_smfmac_f32_32x32x32_bf16 v[96:111], v[188:191], v[136:143], v3\n"
      "buffer_load_dwordx4 v[188:191], v10, s[12:15], s84 offen offset:3072 sc1\n"
      "s_waitcnt vmcnt(9)\n"
      "v_exp_f32 v152, v152\n"
      "v_exp_f32 v153, v153\n"
      "v_exp_f32 v154, v154\n"
      "v_exp_f32 v155, v155\n"
      "v_smfmac_f32_32x32x32_bf16 v[48:63], v[200:203], v[144:151], v3\n"
      "v_exp_f32 v156, v156\n"
      "v_exp_f32 v157, v157\n"
      "v_exp_f32 v158, v158\n"
      "v_exp_f32 v159, v159\n"
      "v_mfma_f32_32x32x16_bf16 v[128:143], v[192:195], v[32:35], 0\n"
      "v_exp_f32 v112, v112\n"
      "v_exp_f32 v113, v113\n"
      "v_exp_f32 v114, v114\n"
      "v_exp_f32 v115, v115\n"
      "v_mfma_f32_32x32x16_bf16 v[128:143], v[196:199], v[36:39], v[128:143]\n"
      "s_waitcnt vmcnt(8)\n"
      "v_exp_f32 v116, v116\n"
      "v_exp_f32 v117, v117\n"
      "v_exp_f32 v118, v118\n"
      "v_exp_f32 v119, v119\n"
      "v_smfmac_f32_32x32x32_bf16 v[48:63], v[204:207], v[152:159], v3\n"
      "v_exp_f32 v120, v120\n"
      "v_exp_f32 v121, v121\n"
      "v_exp_f32 v122, v122\n"
      "v_exp_f32 v123, v123\n"
      "v_smfmac_f32_32x32x32_bf16 v[64:79], v[200:203], v[112:119], v3\n"
      "v_exp_f32 v124, v124\n"
      "v_exp_f32 v125, v125\n"
      "v_exp_f32 v126, v126\n"
      "v_exp_f32 v127, v127\n"
      "v_mfma_f32_32x32x16_bf16 v[144:159], v[192:195], v[40:43], 0\n"
      "s_mov_b32 s84, 57344\n"
      "buffer_load_dwordx4 v[192:195], v10, s[12:15], s84 offen offset:0 sc1\n"
      "v_exp_f32 v128, v128\n"
      "v_exp_f32 v129, v129\n"
      "v_exp_f32 v130, v130\n"
      "v_exp_f32 v131, v131\n"
      "v_mfma_f32_32x32x16_bf16 v[144:159], v[196:199], v[44:47], v[144:159]\n"
      "v_exp_f32 v132, v132\n"
      "v_exp_f32 v133, v133\n"
      "v_exp_f32 v134, v134\n"
      "v_exp_f32 v135, v135\n"
      "v_smfmac_f32_32x32x32_bf16 v[64:79], v[204:207], v[120:127], v3\n"
      "buffer_load_dwordx4 v[196:199], v10, s[12:15], s84 offen offset:1024 sc1\n"
      "v_exp_f32 v136, v136\n"
      "v_exp_f32 v137, v137\n"
      "v_exp_f32 v138, v138\n"
      "v_exp_f32 v139, v139\n"
      "v_smfmac_f32_32x32x32_bf16 v[80:95], v[200:203], v[128:135], v3\n"
      "s_waitcnt vmcnt(9)\n"
      "v_exp_f32 v140, v140\n"
      "v_exp_f32 v141, v141\n"
      "v_exp_f32 v142, v142\n"
      "v_exp_f32 v143, v143\n"
      "v_mfma_f32_32x32x16_bf16 v[112:127], v[160:163], v[16:19], 0\n"
      "s_waitcnt vmcnt(8)\n"
      "v_exp_f32 v144, v144\n"
      "v_exp_f32 v145, v145\n"
      "v_exp_f32 v146, v146\n"
      "v_exp_f32 v147, v147\n"
      "v_mfma_f32_32x32x16_bf16 v[112:127], v[164:167], v[20:23], v[112:127]\n"
      "v_exp_f32 v148, v148\n"
      "v_exp_f32 v149, v149\n"
      "v_exp_f32 v150, v150\n"
      "v_exp_f32 v151, v151\n"
      "v_smfmac_f32_32x32x32_bf16 v[80:95], v[204:207], v[136:143], v3\n"
      "v_exp_f32 v152, v152\n"
      "v_exp_f32 v153, v153\n"
      "v_exp_f32 v154, v154\n"
      "v_exp_f32 v155, v155\n"
      "v_smfmac_f32_32x32x32_bf16 v[96:111], v[200:203], v[144:151], v3\n"
      "v_exp_f32 v156, v156\n"
      "v_exp_f32 v157, v157\n"
      "v_exp_f32 v158, v158\n"
      "v_exp_f32 v159, v159\n"
      "v_mfma_f32_32x32x16_bf16 v[128:143], v[160:163], v[24:27], 0\n"
      "buffer_load_dwordx4 v[200:203], v10, s[12:15], s84 offen offset:2048 sc1\n"
      "v_exp_f32 v112, v112\n"
      "v_exp_f32 v113, v113\n"
      "v_exp_f32 v114, v114\n"
      "v_exp_f32 v115, v115\n"
      "v_mfma_f32_32x32x16_bf16 v[128:143], v[164:167], v[28:31], v[128:143]\n"
      "v_exp_f32 v116, v116\n"
      "v_exp_f32 v117, v117\n"
      "v_exp_f32 v118, v118\n"
      "v_exp_f32 v119, v119\n"
      "v_smfmac_f32_32x32x32_bf16 v[96:111], v[204:207], v[152:159], v3\n"
      "buffer_load_dwordx4 v[204:207], v10, s[12:15], s84 offen offset:3072 sc1\n"
      "s_waitcnt vmcnt(9)\n"
      "v_exp_f32 v120, v120\n"
      "v_exp_f32 v121, v121\n"
      "v_exp_f32 v122, v122\n"
      "v_exp_f32 v123, v123\n"
      "v_smfmac_f32_32x32x32_bf16 v[48:63], v[168:171], v[112:119], v3\n"
      "v_exp_f32 v124, v124\n"
      "v_exp_f32 v125, v125\n"
      "v_exp_f32 v126, v126\n"
      "v_exp_f32 v127, v127\n"
      "v_mfma_f32_32x32x16_bf16 v[144:159], v[160:163], v[32:35], 0\n"
      "v_exp_f32 v128, v128\n"
      "v_exp_f32 v129, v129\n"
      "v_exp_f32 v130, v130\n"
      "v_exp_f32 v131, v131\n"
      "v_mfma_f32_32x32x16_bf16 v[144:159], v[164:167], v[36:39], v[144:159]\n"
      "s_waitcnt vmcnt(8)\n"
      "v_exp_f32 v132, v132\n"
      "v_exp_f32 v133, v133\n"
      "v_exp_f32 v134, v134\n"
      "v_exp_f32 v135, v135\n"
      "v_smfmac_f32_32x32x32_bf16 v[48:63], v[172:175], v[120:127], v3\n"
      "v_exp_f32 v136, v136\n"
      "v_exp_f32 v137, v137\n"
      "v_exp_f32 v138, v138\n"
      "v_exp_f32 v139, v139\n"
      "v_smfmac_f32_32x32x32_bf16 v[64:79], v[168:171], v[128:135], v3\n"
      "v_exp_f32 v140, v140\n"
      "v_exp_f32 v141, v141\n"
      "v_exp_f32 v142, v142\n"
      "v_exp_f32 v143, v143\n"
      "v_mfma_f32_32x32x16_bf16 v[112:127], v[160:163], v[40:43], 0\n"
      "s_mov_b32 s84, 61440\n"
      "buffer_load_dwordx4 v[160:163], v10, s[12:15], s84 offen offset:0 sc1\n"
      "v_exp_f32 v144, v144\n"
      "v_exp_f32 v145, v145\n"
      "v_exp_f32 v146, v146\n"
      "v_exp_f32 v147, v147\n"
      "v_mfma_f32_32x32x16_bf16 v[112:127], v[164:167], v[44:47], v[112:127]\n"
      "v_exp_f32 v148, v148\n"
      "v_exp_f32 v149, v149\n"
      "v_exp_f32 v150, v150\n"
      "v_exp_f32 v151, v151\n"
      "v_smfmac_f32_32x32x32_bf16 v[64:79], v[172:175], v[136:143], v3\n"
      "buffer_load_dwordx4 v[164:167], v10, s[12:15], s84 offen offset:1024 sc1\n"
      "v_exp_f32 v152, v152\n"
      "v_exp_f32 v153, v153\n"
      "v_exp_f32 v154, v154\n"
      "v_exp_f32 v155, v155\n"
      "v_smfmac_f32_32x32x32_bf16 v[80:95], v[168:171], v[144:151], v3\n"
      "s_waitcnt vmcnt(9)\n"
      "v_exp_f32 v156, v156\n"
      "v_exp_f32 v157, v157\n"
      "v_exp_f32 v158, v158\n"
      "v_exp_f32 v159, v159\n"
      "v_mfma_f32_32x32x16_bf16 v[128:143], v[176:179], v[16:19], 0\n"
      "s_waitcnt vmcnt(8)\n"
      "v_exp_f32 v112, v112\n"
      "v_exp_f32 v113, v113\n"
      "v_exp_f32 v114, v114\n"
      "v_exp_f32 v115, v115\n"
      "v_mfma_f32_32x32x16_bf16 v[128:143], v[180:183], v[20:23], v[128:143]\n"
      "v_exp_f32 v116, v116\n"
      "v_exp_f32 v117, v117\n"
      "v_exp_f32 v118, v118\n"
      "v_exp_f32 v119, v119\n"
      "v_smfmac_f32_32x32x32_bf16 v[80:95], v[172:175], v[152:159], v3\n"
      "v_exp_f32 v120, v120\n"
      "v_exp_f32 v121, v121\n"
      "v_exp_f32 v122, v122\n"
      "v_exp_f32 v123, v123\n"
      "v_smfmac_f32_32x32x32_bf16 v[96:111], v[168:171], v[112:119], v3\n"
      "v_exp_f32 v124, v124\n"
      "v_exp_f32 v125, v125\n"
      "v_exp_f32 v126, v126\n"
      "v_exp_f32 v127, v127\n"
      "v_mfma_f32_32x32x16_bf16 v[144:159], v[176:179], v[24:27], 0\n"
      "buffer_load_dwordx4 v[168:171], v10, s[12:15], s84 offen offset:2048 sc1\n"
      "v_exp_f32 v128, v128\n"
      "v_exp_f32 v129, v129\n"
      "v_exp_f32 v130, v130\n"
      "v_exp_f32 v131, v131\n"
      "v_mfma_f32_32x32x16_bf16 v[144:159], v[180:183], v[28:31], v[144:159]\n"
      "v_exp_f32 v132, v132\n"
      "v_exp_f32 v133, v133\n"
      "v_exp_f32 v134, v134\n"
      "v_exp_f32 v135, v135\n"
      "v_smfmac_f32_32x32x32_bf16 v[96:111], v[172:175], v[120:127], v3\n"
      "buffer_load_dwordx4 v[172:175], v10, s[12:15], s84 offen offset:3072 sc1\n"
      "s_waitcnt vmcnt(9)\n"
      "v_exp_f32 v136, v136\n"
      "v_exp_f32 v137, v137\n"
      "v_exp_f32 v138, v138\n"
      "v_exp_f32 v139, v139\n"
      "v_smfmac_f32_32x32x32_bf16 v[48:63], v[184:187], v[128:135], v3\n"
      "v_exp_f32 v140, v140\n"
      "v_exp_f32 v141, v141\n"
      "v_exp_f32 v142, v142\n"
      "v_exp_f32 v143, v143\n"
      "v_mfma_f32_32x32x16_bf16 v[112:127], v[176:179], v[32:35], 0\n"
      "v_exp_f32 v144, v144\n"
      "v_exp_f32 v145, v145\n"
      "v_exp_f32 v146, v146\n"
      "v_exp_f32 v147, v147\n"
      "v_mfma_f32_32x32x16_bf16 v[112:127], v[180:183], v[36:39], v[112:127]\n"
      "s_waitcnt vmcnt(8)\n"
      "v_exp_f32 v148, v148\n"
      "v_exp_f32 v149, v149\n"
      "v_exp_f32 v150, v150\n"
      "v_exp_f32 v151, v151\n"
      "v_smfmac_f32_32x32x32_bf16 v[48:63], v[188:191], v[136:143], v3\n"
      "v_exp_f32 v152, v152\n"
      "v_exp_f32 v153, v153\n"
      "v_exp_f32 v154, v154\n"
      "v_exp_f32 v155, v155\n"
      "v_smfmac_f32_32x32x32_bf16 v[64:79], v[184:187], v[144:151], v3\n"
      "v_exp_f32 v156, v156\n"
      "v_exp_f32 v157, v157\n"
      "v_exp_f32 v158, v158\n"
      "v_exp_f32 v159, v159\n"
      "v_mfma_f32_32x32x16_bf16 v[128:143], v[176:179], v[40:43], 0\n"
      "v_exp_f32 v112, v112\n"
      "v_exp_f32 v113, v113\n"
      "v_exp_f32 v114, v114\n"
      "v_exp_f32 v115, v115\n"
      "v_mfma_f32_32x32x16_bf16 v[128:143], v[180:183], v[44:47], v[128:143]\n"
      "v_exp_f32 v116, v116\n"
      "v_exp_f32 v117, v117\n"
      "v_exp_f32 v118, v118\n"
      "v_exp_f32 v119, v119\n"
      "v_smfmac_f32_32x32x32_bf16 v[64:79], v[188:191], v[152:159], v3\n"
      "v_exp_f32 v120, v120\n"
      "v_exp_f32 v121, v121\n"
      ".Lmk_pad2:\n"
      "s_cbranch_execz .Lmk_padr2\n"
      "s_nop 0\n"
      "v_exp_f32 v122, v122\n"
      "v_exp_f32 v123, v123\n"
      "v_smfmac_f32_32x32x32_bf16 v[80:95], v[184:187], v[112:119], v3\n"
      "s_waitcnt vmcnt(7)\n"
      "v_exp_f32 v124, v124\n"
      "v_exp_f32 v125, v125\n"
      "v_exp_f32 v126, v126\n"
      "v_exp_f32 v127, v127\n"
      "v_mfma_f32_32x32x16_bf16 v[144:159], v[192:195], v[16:19], 0\n"
      "s_waitcnt vmcnt(6)\n"
      "v_exp_f32 v128, v128\n"
      "v_exp_f32 v129, v129\n"
      "v_exp_f32 v130, v130\n"
      "v_exp_f32 v131, v131\n"
      "v_mfma_f32_32x32x16_bf16 v[144:159], v[196:199], v[20:23], v[144:159]\n"
      "v_exp_f32 v132, v132\n"
      "v_exp_f32 v133, v133\n"
      "v_exp_f32 v134, v134\n"
      "v_exp_f32 v135, v135\n"
      "v_smfmac_f32_32x32x32_bf16 v[80:95], v[188:191], v[120:127], v3\n"
      "v_exp_f32 v136, v136\n"
      "v_exp_f32 v137, v137\n"
      "v_exp_f32 v138, v138\n"
      "v_exp_f32 v139, v139\n"
      "v_smfmac_f32_32x32x32_bf16 v[96:111], v[184:187], v[128:135], v3\n"
      "s_waitcnt vmcnt(3)\n"
      "v_exp_f32 v140, v140\n"
      "v_exp_f32 v141, v141\n"
      "v_exp_f32 v142, v142\n"
      "v_exp_f32 v143, v143\n"
      "v_mfma_f32_32x32x16_bf16 v[112:127], v[160:163], v[16:19], 0\n"
      "s_waitcnt vmcnt(2)\n"
      "v_exp_f32 v144, v144\n"
      "v_exp_f32 v145, v145\n"
      "v_exp_f32 v146, v146\n"
      "v_exp_f32 v147, v147\n"
      "v_mfma_f32_32x32x16_bf16 v[112:127], v[164:167], v[20:23], v[112:127]\n"
      "v_exp_f32 v148, v148\n"
      "v_exp_f32 v149, v149\n"
      "v_exp_f32 v150, v150\n"
      "v_exp_f32 v151, v151\n"
      "v_smfmac_f32_32x32x32_bf16 v[96:111], v[188:191], v[136:143], v3\n"
      "v_exp_f32 v152, v152\n"
      "v_exp_f32 v153, v153\n"
      "v_exp_f32 v154, v154\n"
      "v_exp_f32 v155, v155\n"
      "v_smfmac_f32_32x32x32_bf16 v[48:63], v[200:203], v[144:151], v3\n"
      "v_exp_f32 v156, v156\n"
      "v_exp_f32 v157, v157\n"
      "v_exp_f32 v158, v158\n"
      "v_exp_f32 v159, v159\n"
      "v_mfma_f32_32x32x16_bf16 v[128:143], v[192:195], v[24:27], 0\n"
      "v_exp_f32 v112, v112\n"
      "v_exp_f32 v113, v113\n"
      "v_exp_f32 v114, v114\n"
      "v_exp_f32 v115, v115\n"
      "v_mfma_f32_32x32x16_bf16 v[128:143], v[196:199], v[28:31], v[128:143]\n"
      "v_exp_f32 v116, v116\n"
      "v_exp_f32 v117, v117\n"
      "v_exp_f32 v118, v118\n"
      "v_exp_f32 v119, v119\n"
      "v_smfmac_f32_32x32x32_bf16 v[48:63], v[204:207], v[152:159], v3\n"
      "s_waitcnt vmcnt(1)\n"
      "v_exp_f32 v120, v120\n"
      "v_exp_f32 v121, v121\n"
      "v_exp_f32 v122, v122\n"
      "v_exp_f32 v123, v123\n"
      "v_smfmac_f32_32x32x32_bf16 v[48:63], v[168:171], v[112:119], v3\n"
      "v_exp_f32 v124, v124\n"
      "v_exp_f32 v125, v125\n"
      "v_exp_f32 v126, v126\n"
      "v_exp_f32 v127, v127\n"
      "v_mfma_f32_32x32x16_bf16 v[144:159], v[160:163], v[24:27], 0\n"
      "v_exp_f32 v128, v128\n"
      "v_exp_f32 v129, v129\n"
      "v_exp_f32 v130, v130\n"
      "v_exp_f32 v131, v131\n"
      "v_mfma_f32_32x32x16_bf16 v[144:159], v[164:167], v[28:31], v[144:159]\n"
      "s_waitcnt vmcnt(0)\n"
      "v_exp_f32 v132, v132\n"
      "v_exp_f32 v133, v133\n"
      "v_exp_f32 v134, v134\n"
      "v_exp_f32 v135, v135\n"
      "v_smfmac_f32_32x32x32_bf16 v[48:63], v[172:175], v[120:127], v3\n"
      "v_exp_f32 v136, v136\n"
      "v_exp_f32 v137, v137\n"
      "v_exp_f32 v138, v138\n"
      "v_exp_f32 v139, v139\n"
      "v_smfmac_f32_32x32x32_bf16 v[64:79], v[200:203], v[128:135], v3\n"
      "v_exp_f32 v140, v140\n"
      "v_exp_f32 v141, v141\n"
      "v_exp_f32 v142, v142\n"
      "v_exp_f32 v143, v143\n"
      "v_mfma_f32_32x32x16_bf16 v[112:127], v[192:195], v[32:35], 0\n"
      "v_exp_f32 v144, v144\n"
      "v_exp_f32 v145, v145\n"
      "v_exp_f32 v146, v146\n"
      "v_exp_f32 v147, v147\n"
      "v_mfma_f32_32x32x16_bf16 v[112:127], v[196:199], v[36:39], v[112:127]\n"
      "ds_write_b128 v4, v[48:51] offset:0\n"
      "v_exp_f32 v148, v148\n"
      "v_exp_f32 v149, v149\n"
      "v_exp_f32 v150, v150\n"
      "v_exp_f32 v151, v151\n"
      "v_smfmac_f32_32x32x32_bf16 v[64:79], v[204:207], v[136:143], v3\n"
      "v_exp_f32 v152, v152\n"
      "v_exp_f32 v153, v153\n"
      "v_exp_f32 v154, v154\n"
      "v_exp_f32 v155, v155\n"
      "v_smfmac_f32_32x32x32_bf16 v[64:79], v[168:171], v[144:151], v3\n"
      "ds_write_b128 v4, v[52:55] offset:1024\n"
      "v_exp_f32 v156, v156\n"
      "v_exp_f32 v157, v157\n"
      "v_exp_f32 v158, v158\n"
      "v_exp_f32 v159, v159\n"
      "v_mfma_f32_32x32x16_bf16 v[128:143], v[160:163], v[32:35], 0\n"
      "v_exp_f32 v112, v112\n"
      "v_exp_f32 v113, v113\n"
      "v_exp_f32 v114, v114\n"
      "v_exp_f32 v115, v115\n"
      "v_mfma_f32_32x32x16_bf16 v[128:143], v[164:167], v[36:39], v[128:143]\n"
      "ds_write_b128 v4, v[56:59] offset:2048\n"
      "v_exp_f32 v116, v116\n"
      "v_exp_f32 v117, v117\n"
      "v_exp_f32 v118, v118\n"
      "v_exp_f32 v119, v119\n"
      "v_smfmac_f32_32x32x32_bf16 v[64:79], v[172:175], v[152:159], v3\n"
      "v_exp_f32 v120, v120\n"
      "v_exp_f32 v121, v121\n"
      "v_exp_f32 v122, v122\n"
      "v_exp_f32 v123, v123\n"
      "v_smfmac_f32_32x32x32_bf16 v[80:95], v[200:203], v[112:119], v3\n"
      "ds_write_b128 v4, v[60:63] offset:3072\n"
      "v_exp_f32 v124, v124\n"
      "v_exp_f32 v125, v125\n"
      "v_exp_f32 v126, v126\n"
      "v_exp_f32 v127, v127\n"
      "v_mfma_f32_32x32x16_bf16 v[144:159], v[192:195], v[40:43], 0\n"
      "v_exp_f32 v128, v128\n"
      "v_exp_f32 v129, v129\n"
      "v_exp_f32 v130, v130\n"
      "v_exp_f32 v131, v131\n"
      "v_mfma_f32_32x32x16_bf16 v[144:159], v[196:199], v[44:47], v[144:159]\n"
      "ds_write_b128 v5, v[64:67] offset:0\n"
      "v_exp_f32 v132, v132\n"
      "v_exp_f32 v133, v133\n"
      "v_exp_f32 v134, v134\n"
      "v_exp_f32 v135, v135\n"
      "v_smfmac_f32_32x32x32_bf16 v[80:95], v[204:207], v[120:127], v3\n"
      "ds_write_b128 v5, v[68:71] offset:1024\n"
      "v_exp_f32 v136, v136\n"
      "v_exp_f32 v137, v137\n"
      "v_exp_f32 v138, v138\n"
      "v_exp_f32 v139, v139\n"
      "v_smfmac_f32_32x32x32_bf16 v[80:95], v[168:171], v[128:135], v3\n"
      "ds_write_b128 v5, v[72:75] offset:2048\n"
      "v_exp_f32 v140, v140\n"
      "v_exp_f32 v141, v141\n"
      "v_exp_f32 v142, v142\n"
      "v_exp_f32 v143, v143\n"
      "v_exp_f32 v144, v144\n"
      "v_exp_f32 v145, v145\n"
      "v_smfmac_f32_32x32x32_bf16 v[80:95], v[172:175], v[136:143], v3\n"
      "v_exp_f32 v146, v146\n"
      "v_exp_f32 v147, v147\n"
      "v_mfma_f32_32x32x16_bf16 v[112:127], v[160:163], v[40:43], 0\n"
      "ds_write_b128 v5, v[76:79] offset:3072\n"
      "v_exp_f32 v148, v148\n"
      "v_exp_f32 v149, v149\n"
      "v_exp_f32 v150, v150\n"
      "v_exp_f32 v151, v151\n"
      "v_mfma_f32_32x32x16_bf16 v[112:127], v[164:167], v[44:47], v[112:127]\n"
      "v_exp_f32 v152, v152\n"
      "v_exp_f32 v153, v153\n"
      "v_exp_f32 v154, v154\n"
      "v_exp_f32 v155, v155\n"
      "v_smfmac_f32_32x32x32_bf16 v[96:111], v[200:203], v[144:151], v3\n"
      "ds_write_b128 v6, v[80:83] offset:0\n"
      "v_exp_f32 v156, v156\n"
      "v_exp_f32 v157, v157\n"
      "v_exp_f32 v158, v158\n"
      "v_exp_f32 v159, v159\n"
      "ds_write_b128 v6, v[84:87] offset:1024\n"
      "s_nop 0\n"
      "v_exp_f32 v112, v112\n"
      "v_exp_f32 v113, v113\n"
      "v_exp_f32 v114, v114\n"
      "v_exp_f32 v115, v115\n"
      "ds_write_b128 v6, v[88:91] offset:2048\n"
      "v_exp_f32 v116, v116\n"
      "v_exp_f32 v117, v117\n"
      "v_exp_f32 v118, v118\n"
      "v_exp_f32 v119, v119\n"
      "v_smfmac_f32_32x32x32_bf16 v[96:111], v[204:207], v[152:159], v3\n"
      "ds_write_b128 v6, v[92:95] offset:3072\n"
      "v_exp_f32 v120, v120\n"
      "v_exp_f32 v121, v121\n"
      "v_exp_f32 v122, v122\n"
      "v_exp_f32 v123, v123\n"
      "v_smfmac_f32_32x32x32_bf16 v[96:111], v[168:171], v[112:119], v3\n"
      "v_exp_f32 v124, v124\n"
      "v_exp_f32 v125, v125\n"
      "v_exp_f32 v126, v126\n"
      "v_exp_f32_e64 v127, v127\n"
      "s_nop 1\n"
      "v_smfmac_f32_32x32x32_bf16 v[96:111], v[172:175], v[120:127], v3\n"
      "s_waitcnt lgkmcnt(0)\n"
      "s_barrier\n"
      "s_setprio 3\n"
      "ds_read_b128 v[48:51], v2 offset:0\n"
      "ds_read_b128 v[52:55], v2 offset:1024\n"
      "ds_read_b128 v[56:59], v2 offset:2048\n"
      "ds_read_b128 v[60:63], v2 offset:3072\n"
      "ds_read_b128 v[64:67], v2 offset:4096\n"
      "ds_read_b128 v[68:71], v2 offset:5120\n"
      "ds_read_b128 v[72:75], v2 offset:6144\n"
      "ds_read_b128 v[76:79], v2 offset:7168\n"
      "ds_read_b128 v[80:83], v2 offset:8192\n"
      "ds_read_b128 v[84:87], v2 offset:9216\n"
      "ds_read_b128 v[88:91], v2 offset:10240\n"
      "ds_read_b128 v[92:95], v2 offset:11264\n"
      "s_lshl2_add_u32 s40, s31, s3\n"
      "s_lshl_b32 s40, s40, 4\n"
      "s_add_u32 s40, s40, s43\n"
      "s_lshl_b32 s40, s40, 11\n"
      "s_add_u32 s46, s40, 9437184\n"
      "s_and_b32 s40, s2, 7\n"
      "s_lshr_b32 s41, s2, 5\n"
      "s_lshl3_add_u32 s53, s40, s41\n"
      "s_lshl2_add_u32 s40, s53, s3\n"
      "s_lshl_b32 s40, s40, 13\n"
      "s_add_u32 s48, s40, 9437184\n"
      "s_add_u32 s49, s48, 4096\n"
      "s_waitcnt lgkmcnt(11)\n"
      "v_pk_add_f32 v[96:97], v[96:97], v[48:49]\n"
      "v_pk_add_f32 v[98:99], v[98:99], v[50:51]\n"
      "s_waitcnt lgkmcnt(10)\n"
      "v_pk_add_f32 v[100:101], v[100:101], v[52:53]\n"
      "v_pk_add_f32 v[102:103], v[102:103], v[54:55]\n"
      "s_waitcnt lgkmcnt(9)\n"
      "v_pk_add_f32 v[104:105], v[104:105], v[56:57]\n"
      "v_pk_add_f32 v[106:107], v[106:107], v[58:59]\n"
      "s_waitcnt lgkmcnt(8)\n"
      "v_pk_add_f32 v[108:109], v[108:109], v[60:61]\n"
      "v_pk_add_f32 v[110:111], v[110:111], v[62:63]\n"
      "s_waitcnt lgkmcnt(7)\n"
      "v_pk_add_f32 v[96:97], v[96:97], v[64:65]\n"
      "v_pk_add_f32 v[98:99], v[98:99], v[66:67]\n"
      "s_waitcnt lgkmcnt(6)\n"
      "v_pk_add_f32 v[100:101], v[100:101], v[68:69]\n"
      "v_pk_add_f32 v[102:103], v[102:103], v[70:71]\n"
      "s_waitcnt lgkmcnt(5)\n"
      "v_pk_add_f32 v[104:105], v[104:105], v[72:73]\n"
      "v_pk_add_f32 v[106:107], v[106:107], v[74:75]\n"
      "s_waitcnt lgkmcnt(4)\n"
      "v_pk_add_f32 v[108:109], v[108:109], v[76:77]\n"
      "v_pk_add_f32 v[110:111], v[110:111], v[78:79]\n"
      "s_waitcnt lgkmcnt(3)\n"
      "v_pk_add_f32 v[96:97], v[96:97], v[80:81]\n"
      "v_pk_add_f32 v[98:99], v[98:99], v[82:83]\n"
      "s_waitcnt lgkmcnt(2)\n"
      "v_pk_add_f32 v[100:101], v[100:101], v[84:85]\n"
      "v_pk_add_f32 v[102:103], v[102:103], v[86:87]\n"
      "s_waitcnt lgkmcnt(1)\n"
      "v_pk_add_f32 v[104:105], v[104:105], v[88:89]\n"
      "v_pk_add_f32 v[106:107], v[106:107], v[90:91]\n"
      "s_waitcnt lgkmcnt(0)\n"
      "v_pk_add_f32 v[108:109], v[108:109], v[92:93]\n"
      "v_pk_add_f32 v[110:111], v[110:111], v[94:95]\n"
      "s_barrier\n"
      "v_mov_b32 v112, v108\n"
      "v_mov_b32 v113, v108\n"
      "v_maximum3_f32 v114, abs(v96), abs(v97), abs(v98)\n"
      "v_maximum3_f32 v114, v114, abs(v99), abs(v100)\n"
      "v_permlane32_swap_b32 v112, v113\n"
      "v_rcp_f32 v112, v113\n"
      "v_maximum3_f32 v114, v114, abs(v101), abs(v102)\n"
      "v_maximum3_f32 v114, v114, abs(v103), abs(v104)\n"
      "v_maximum3_f32 v114, v114, abs(v105), abs(v106)\n"
      "v_maximum3_f32 v114, v114, abs(v107), abs(v108)\n"
      "v_maximum3_f32 v114, v114, abs(v109), abs(v110)\n"
      "v_maximum3_f32 v114, v114, v114, abs(v111)\n"
      "v_cmp_nlt_f32 vcc, 0xe000000, v113\n"
      "s_mov_b32 s40, 0x76800000\n"
      "v_cmp_ngt_f32 s[50:51], s40, v114\n"
      "s_mov_b32 s41, 0x7800000\n"
      "v_cmp_gt_f32 s[44:45], s41, v114\n"
      "v_mul_f32 v116, v112, v96\n"
      "v_mul_f32 v117, v112, v97\n"
      "v_mul_f32 v118, v112, v98\n"
      "v_mul_f32 v119, v112, v99\n"
      "v_mul_f32 v120, v112, v100\n"
      "v_mul_f32 v121, v112, v101\n"
      "v_mul_f32 v122, v112, v102\n"
      "v_mul_f32_e64 v123, v112, v103\n"
      "s_or_b64 vcc, vcc, s[44:45]\n"
      "s_or_b64 vcc, vcc, s[50:51]\n"
      "s_cbranch_scc1 .Lmk_exact\n"
      ".Lmk_norm2:\n"
      "v_cvt_pk_bf16_f32 v132, v116, v117\n"
      "v_cvt_pk_bf16_f32 v133, v118, v119\n"
      "v_cvt_pk_bf16_f32 v134, v120, v121\n"
      "v_cvt_pk_bf16_f32 v135, v122, v123\n"
      "v_min_u32 v132, -2, v132\n"
      "v_min_u32 v133, -2, v133\n"
      "v_min_u32 v134, -2, v134\n"
      "v_min_u32 v135, -2, v135\n"
      "buffer_store_dwordx4 v[132:135], v8, s[12:15], s46 offen sc1\n"
      "v_mul_f32 v124, v112, v104\n"
      "v_mul_f32 v125, v112, v105\n"
      "v_mul_f32 v126, v112, v106\n"
      "v_mul_f32 v127, v112, v107\n"
      "v_mul_f32 v128, v112, v108\n"
      "v_mul_f32 v129, v112, v109\n"
      "v_mul_f32 v130, v112, v110\n"
      "v_mul_f32 v131, v112, v111\n"
      "v_cvt_pk_bf16_f32 v136, v124, v125\n"
      "v_cvt_pk_bf16_f32 v137, v126, v127\n"
      "v_cvt_pk_bf16_f32 v138, v128, v129\n"
      "v_cvt_pk_bf16_f32 v139, v130, v131\n"
      "v_cndmask_b32 v138, v138, 0, s[80:81]\n"
      "v_cndmask_b32 v139, v139, 0, s[80:81]\n"
      "v_min_u32 v136, -2, v136\n"
      "v_min_u32 v137, -2, v137\n"
      "v_min_u32 v138, -2, v138\n"
      "v_min_u32 v139, -2, v139\n"
      "buffer_store_dwordx4 v[136:139], v8, s[12:15], s46 offen offset:1024 sc1\n"
      "s_setprio 1\n"
      "s_mov_b32 s95, 131072\n"
      "v_lshrrev_b32 v12, 4, v1\n"
      "v_and_b32 v13, 15, v1\n"
      "s_lshl_b32 s40, s3, 3\n"
      "v_add_u32_e64 v12, s40, v12\n"
      "v_mul_u32_u24 v16, 272, v12\n"
      "v_lshl_add_u32 v16, v13, 4, v16\n"
      "v_add_u32 v16, 139264, v16\n"
      "v_mul_u32_u24 v17, 0xe00, v12\n"
      "v_lshl_add_u32 v17, v13, 4, v17\n"
      "s_bfe_u32 s41, s2, 0x20003\n"
      "s_cmp_eq_u32 s41, 3\n"
      "s_cselect_b32 s40, 8, 16\n"
      "v_cmp_gt_u32 vcc, s40, v13\n"
      "v_mov_b32 v18, 2147483647\n"
      "s_lshr_b32 s44, s53, 1\n"
      "s_lshl_b32 s44, s44, 6\n"
      "s_and_b32 s45, s53, 1\n"
      "s_add_u32 s44, s44, s45\n"
      "s_mul_i32 s44, s44, 1792\n"
      "s_lshl_b32 s41, s41, 8\n"
      "s_add_u32 s44, s44, s41\n"
      "v_cndmask_b32 v17, v18, v17, vcc\n"
      "s_add_u32 s45, s44, 14336\n"
      "s_mov_b64 s[16:17], s[24:25]\n"
      "s_mov_b32 s18, 3670016\n"
      "v_mov_b32 v19, 161796\n"
      "s_sleep 11\n"
      "s_mov_b32 s54, 15\n"
      "s_nop 0\n"
      "buffer_load_dwordx4 v[176:179], v8, s[12:15], s48 offen offset:0 sc1\n"
      "buffer_load_dwordx4 v[180:183], v8, s[12:15], s48 offen offset:1024 sc1\n"
      "buffer_load_dwordx4 v[184:187], v8, s[12:15], s48 offen offset:2048 sc1\n"
      "buffer_load_dwordx4 v[188:191], v8, s[12:15], s48 offen offset:3072 sc1\n"
      "buffer_load_dwordx4 v[192:195], v8, s[12:15], s49 offen offset:0 sc1\n"
      "buffer_load_dwordx4 v[196:199], v8, s[12:15], s49 offen offset:1024 sc1\n"
      "buffer_load_dwordx4 v[200:203], v8, s[12:15], s49 offen offset:2048 sc1\n"
      "buffer_load_dwordx4 v[204:207], v8, s[12:15], s49 offen offset:3072 sc1\n"
      "s_waitcnt vmcnt(6)\n"
      "v_max3_u32 v3, v176, v177, v178\n"
      "v_max3_u32 v4, v179, v180, v181\n"
      "v_max3_u32 v3, v3, v182, v183\n"
      "v_max_u32 v3, v3, v4\n"
      "v_cmp_eq_u32 vcc, -1, v3\n"
      "s_cbranch_vccnz .Lmk_obk17\n"
      "ds_write_b128 v2, v[176:179] offset:0\n"
      "ds_write_b128 v2, v[180:183] offset:1024\n"
      "s_bitset0_b32 s54, 0\n"
      ".Lmk_obk17:\n"
      "s_waitcnt vmcnt(4)\n"
      "v_max3_u32 v3, v184, v185, v186\n"
      "v_max3_u32 v4, v187, v188, v189\n"
      "v_max3_u32 v3, v3, v190, v191\n"
      "v_max_u32_e64 v3, v3, v4\n"
      "v_cmp_eq_u32 vcc, -1, v3\n"
      "s_cbranch_vccnz .Lmk_obk18\n"
      "ds_write_b128 v2, v[184:187] offset:2048\n"
      "ds_write_b128 v2, v[188:191] offset:3072\n"
      "s_bitset0_b32 s54, 1\n"
      ".Lmk_obk18:\n"
      "s_waitcnt vmcnt(2)\n"
      "v_max3_u32 v3, v192, v193, v194\n"
      "v_max3_u32 v4, v195, v196, v197\n"
      "v_max3_u32 v3, v3, v198, v199\n"
      "v_max_u32_e64 v3, v3, v4\n"
      "v_cmp_eq_u32 vcc, -1, v3\n"
      "s_cbranch_vccnz .Lmk_obk19\n"
      "ds_write_b128 v2, v[192:195] offset:4096\n"
      "ds_write_b128 v2, v[196:199] offset:5120\n"
      "s_bitset0_b32 s54, 2\n"
      ".Lmk_obk19:\n"
      "s_waitcnt vmcnt(0)\n"
      "v_max3_u32 v3, v200, v201, v202\n"
      "v_max3_u32 v4, v203, v204, v205\n"
      "v_max3_u32 v3, v3, v206, v207\n"
      "v_max_u32_e64 v3, v3, v4\n"
      "v_cmp_eq_u32 vcc, -1, v3\n"
      "s_cbranch_vccnz .Lmk_obk20\n"
      "ds_write_b128 v2, v[200:203] offset:6144\n"
      "ds_write_b128 v2, v[204:207] offset:7168\n"
      "s_bitset0_b32 s54, 3\n"
      ".Lmk_obk20:\n"
      ".Lmk_obp:\n"
      "s_cmp_eq_u32 s54, 0\n"
      "s_cbranch_scc1 .Lmk_obd\n"
      "s_bitcmp0_b32 s54, 0\n"
      "s_cbranch_scc1 .Lmk_obl21\n"
      "buffer_load_dwordx4 v[176:179], v8, s[12:15], s48 offen offset:0 sc1\n"
      "buffer_load_dwordx4 v[180:183], v8, s[12:15], s48 offen offset:1024 sc1\n"
      ".Lmk_obl21:\n"
      "s_bitcmp0_b32 s54, 1\n"
      "s_cbranch_scc1 .Lmk_obl22\n"
      "buffer_load_dwordx4 v[184:187], v8, s[12:15], s48 offen offset:2048 sc1\n"
      "buffer_load_dwordx4 v[188:191], v8, s[12:15], s48 offen offset:3072 sc1\n"
      ".Lmk_obl22:\n"
      "s_bitcmp0_b32 s54, 2\n"
      "s_cbranch_scc1 .Lmk_obl23\n"
      "buffer_load_dwordx4 v[192:195], v8, s[12:15], s49 offen offset:0 sc1\n"
      "buffer_load_dwordx4 v[196:199], v8, s[12:15], s49 offen offset:1024 sc1\n"
      ".Lmk_obl23:\n"
      "s_bitcmp0_b32 s54, 3\n"
      "s_cbranch_scc1 .Lmk_obl24\n"
      "buffer_load_dwordx4 v[200:203], v8, s[12:15], s49 offen offset:2048 sc1\n"
      "buffer_load_dwordx4 v[204:207], v8, s[12:15], s49 offen offset:3072 sc1\n"
      ".Lmk_obl24:\n"
      "s_waitcnt vmcnt(0)\n"
      "s_bitcmp0_b32 s54, 0\n"
      "s_cbranch_scc1 .Lmk_obm25\n"
      "v_max3_u32 v3, v176, v177, v178\n"
      "v_max3_u32 v4, v179, v180, v181\n"
      "v_max3_u32 v3, v3, v182, v183\n"
      "v_max_u32 v3, v3, v4\n"
      "v_cmp_eq_u32 vcc, -1, v3\n"
      "s_cbranch_vccnz .Lmk_obk26\n"
      "ds_write_b128 v2, v[176:179] offset:0\n"
      "ds_write_b128 v2, v[180:183] offset:1024\n"
      "s_bitset0_b32 s54, 0\n"
      ".Lmk_obk26:\n"
      ".Lmk_obm25:\n"
      "s_bitcmp0_b32 s54, 1\n"
      "s_cbranch_scc1 .Lmk_obm27\n"
      "v_max3_u32 v3, v184, v185, v186\n"
      "v_max3_u32 v4, v187, v188, v189\n"
      "v_max3_u32 v3, v3, v190, v191\n"
      "v_max_u32 v3, v3, v4\n"
      "v_cmp_eq_u32 vcc, -1, v3\n"
      "s_cbranch_vccnz .Lmk_obk28\n"
      "ds_write_b128 v2, v[184:187] offset:2048\n"
      "ds_write_b128 v2, v[188:191] offset:3072\n"
      "s_bitset0_b32 s54, 1\n"
      ".Lmk_obk28:\n"
      ".Lmk_obm27:\n"
      "s_bitcmp0_b32 s54, 2\n"
      "s_cbranch_scc1 .Lmk_obm29\n"
      "v_max3_u32 v3, v192, v193, v194\n"
      "v_max3_u32 v4, v195, v196, v197\n"
      "v_max3_u32 v3, v3, v198, v199\n"
      "v_max_u32 v3, v3, v4\n"
      "v_cmp_eq_u32 vcc, -1, v3\n"
      "s_cbranch_vccnz .Lmk_obk30\n"
      "ds_write_b128 v2, v[192:195] offset:4096\n"
      "ds_write_b128 v2, v[196:199] offset:5120\n"
      "s_bitset0_b32 s54, 2\n"
      ".Lmk_obk30:\n"
      ".Lmk_obm29:\n"
      "s_bitcmp0_b32 s54, 3\n"
      "s_cbranch_scc1 .Lmk_obm31\n"
      "v_max3_u32 v3, v200, v201, v202\n"
      "v_max3_u32 v4, v203, v204, v205\n"
      "v_max3_u32 v3, v3, v206, v207\n"
      "v_max_u32 v3, v3, v4\n"
      "v_cmp_eq_u32 vcc, -1, v3\n"
      "s_cbranch_vccnz .Lmk_obk32\n"
      "ds_write_b128 v2, v[200:203] offset:6144\n"
      "ds_write_b128 v2, v[204:207] offset:7168\n"
      "s_bitset0_b32 s54, 3\n"
      ".Lmk_obk32:\n"
      ".Lmk_obm31:\n"
      "s_sub_u32 s95, s95, 1\n"
      "s_cbranch_scc0 .Lmk_obp\n"
      ".Lmk_obd:\n"
      "ds_write_b32 v14, v14 offset:16\n"
      "s_mov_b32 s95, 1048576\n"
      ".Lmk_taw:\n"
      "ds_read_b32 v20, v19\n"
      "s_waitcnt lgkmcnt(0)\n"
      "v_cmp_gt_u32 vcc, 4, v20\n"
      "s_cbranch_vccz .Lmk_tad\n"
      "s_sleep 1\n"
      "s_sub_u32 s95, s95, 1\n"
      "s_cbranch_scc0 .Lmk_taw\n"
      ".Lmk_tad:\n"
      "ds_read_b128 v[24:27], v16\n"
      "ds_read_b128 v[28:31], v16 offset:1088\n"
      "s_waitcnt lgkmcnt(1)\n"
      "buffer_store_dwordx4 v[24:27], v17, s[16:19], s44 offen sc1\n"
      "s_waitcnt lgkmcnt(0)\n"
      "buffer_store_dwordx4 v[28:31], v17, s[16:19], s45 offen sc1\n"
      "s_barrier\n"
      "s_endpgm\n"
      ".Lmk_Lw:\n"
      "ds_read_b32 v15, v14\n"
      "s_sub_u32 s95, s95, 1\n"
      "s_cbranch_scc0 .Lmk_Lr\n"
      "s_branch .Lmk_Lr2\n"
      ".Lmk_exact:\n"
      "v_cndmask_b32 v46, v46, 0, s[80:81]\n"
      "s_lshl_b32 s40, s43, 18\n"
      "s_add_u32 s40, s40, 3145728\n"
      "s_mov_b32 s41, 63\n"
      "v_mov_b32 v96, 0\n"
      "v_mov_b32 v97, 0\n"
      "v_mov_b32 v98, 0\n"
      "v_mov_b32 v99, 0\n"
      "v_mov_b32 v100, 0\n"
      "v_mov_b32 v101, 0\n"
      "v_mov_b32 v102, 0\n"
      "v_mov_b32 v103, 0\n"
      "v_mov_b32 v104, 0\n"
      "v_mov_b32 v105, 0\n"
      "v_mov_b32 v106, 0\n"
      "v_mov_b32 v107, 0\n"
      "v_mov_b32 v108, 0\n"
      "v_mov_b32 v109, 0\n"
      "v_mov_b32 v110, 0\n"
      "v_mov_b32_e64 v111, 0\n"
      "v_mov_b32 v48, 0xff61b1e6\n"
      ".Lmk_exl:\n"
      "buffer_load_dwordx4 v[160:163], v8, s[12:15], s40 offen offset:0 sc1\n"
      "buffer_load_dwordx4 v[164:167], v8, s[12:15], s40 offen offset:1024 sc1\n"
      "buffer_load_dwordx4 v[168:171], v8, s[12:15], s40 offen offset:2048 sc1\n"
      "buffer_load_dwordx4 v[172:175], v8, s[12:15], s40 offen offset:3072 sc1\n"
      "s_waitcnt vmcnt(0)\n"
      "v_mfma_f32_32x32x16_bf16 v[112:127], v[160:163], v[40:43], 0\n"
      "v_mfma_f32_32x32x16_bf16 v[112:127], v[164:167], v[44:47], v[112:127]\n"
      "v_mov_b32_e64 v49, v48\n"
      "s_nop 10\n"
      "v_max3_f32 v49, v49, v112, v113\n"
      "v_max3_f32 v49, v49, v114, v115\n"
      "v_max3_f32 v49, v49, v116, v117\n"
      "v_max3_f32 v49, v49, v118, v119\n"
      "v_max3_f32 v49, v49, v120, v121\n"
      "v_max3_f32 v49, v49, v122, v123\n"
      "v_max3_f32 v49, v49, v124, v125\n"
      "v_max3_f32 v49, v49, v126, v127\n"
      "v_mov_b32 v50, v49\n"
      "s_nop 1\n"
      "v_permlane32_swap_b32 v49, v50\n"
      "v_max_f32 v49, v49, v50\n"
      "v_sub_f32 v50, v48, v49\n"
      "v_exp_f32 v50, v50\n"
      "v_mov_b32 v48, v49\n"
      "v_sub_f32 v112, v112, v49\n"
      "v_sub_f32 v113, v113, v49\n"
      "v_sub_f32 v114, v114, v49\n"
      "v_sub_f32 v115, v115, v49\n"
      "v_sub_f32 v116, v116, v49\n"
      "v_sub_f32 v117, v117, v49\n"
      "v_sub_f32 v118, v118, v49\n"
      "v_sub_f32 v119, v119, v49\n"
      "v_sub_f32 v120, v120, v49\n"
      "v_sub_f32 v121, v121, v49\n"
      "v_sub_f32 v122, v122, v49\n"
      "v_sub_f32 v123, v123, v49\n"
      "v_sub_f32 v124, v124, v49\n"
      "v_sub_f32 v125, v125, v49\n"
      "v_sub_f32 v126, v126, v49\n"
      "v_sub_f32 v127, v127, v49\n"
      "v_exp_f32 v112, v112\n"
      "v_exp_f32 v113, v113\n"
      "v_exp_f32 v114, v114\n"
      "v_exp_f32 v115, v115\n"
      "v_exp_f32 v116, v116\n"
      "v_exp_f32 v117, v117\n"
      "v_exp_f32 v118, v118\n"
      "v_exp_f32 v119, v119\n"
      "v_exp_f32 v120, v120\n"
      "v_exp_f32 v121, v121\n"
      "v_exp_f32 v122, v122\n"
      "v_exp_f32 v123, v123\n"
      "v_exp_f32 v124, v124\n"
      "v_exp_f32 v125, v125\n"
      "v_exp_f32 v126, v126\n"
      "v_exp_f32 v127, v127\n"
      "v_mul_f32 v96, v50, v96\n"
      "v_mul_f32 v97, v50, v97\n"
      "v_mul_f32 v98, v50, v98\n"
      "v_mul_f32 v99, v50, v99\n"
      "v_mul_f32 v100, v50, v100\n"
      "v_mul_f32 v101, v50, v101\n"
      "v_mul_f32 v102, v50, v102\n"
      "v_mul_f32 v103, v50, v103\n"
      "v_mul_f32 v104, v50, v104\n"
      "v_mul_f32 v105, v50, v105\n"
      "v_mul_f32 v106, v50, v106\n"
      "v_mul_f32 v107, v50, v107\n"
      "v_mul_f32 v108, v50, v108\n"
      "v_mul_f32 v109, v50, v109\n"
      "v_mul_f32 v110, v50, v110\n"
      "v_mul_f32 v111, v50, v111\n"
      "s_nop 1\n"
      "v_smfmac_f32_32x32x32_bf16 v[96:111], v[168:171], v[112:119], v3\n"
      "v_smfmac_f32_32x32x32_bf16 v[96:111], v[172:175], v[120:127], v3\n"
      "s_add_u32 s40, s40, 0x1000\n"
      "s_sub_u32 s41, s41, 1\n"
      "s_cbranch_scc0 .Lmk_exl\n"
      "s_nop 8\n"
      "v_mov_b32 v112, v108\n"
      "v_mov_b32 v113, v108\n"
      "s_nop 1\n"
      "v_permlane32_swap_b32 v112, v113\n"
      "v_rcp_f32 v112, v113\n"
      "s_nop 0\n"
      "v_mul_f32 v116, v112, v96\n"
      "v_mul_f32 v117, v112, v97\n"
      "v_mul_f32 v118, v112, v98\n"
      "v_mul_f32 v119, v112, v99\n"
      "v_mul_f32 v120, v112, v100\n"
      "v_mul_f32 v121, v112, v101\n"
      "v_mul_f32 v122, v112, v102\n"
      "v_mul_f32 v123, v112, v103\n"
      "s_branch .Lmk_norm2\n"
      ".Lmk_setA:\n"
      "v_and_b32 v0, 16, v1\n"
      "v_lshlrev_b32_e64 v0, 20, v0\n"
      "s_lshl1_add_u32 s42, s34, s39\n"
      "s_lshl1_add_u32 s40, s33, s38\n"
      "s_lshl_b32 s41, s40, 4\n"
      "s_add_u32 s41, s41, s42\n"
      "s_lshl_b32 s41, s41, 7\n"
      "s_add_u32 s41, s41, 397312\n"
      "v_mov_b32 v9, 2147483647\n"
      "v_writelane_b32 v9, s41, 0\n"
      "s_lshl_b32 s41, s40, 5\n"
      "s_add_u32 s41, s41, s42\n"
      "s_lshl_b32 s41, s41, 11\n"
      "s_add_u32 s64, s41, 7340032\n"
      "s_add_u32 s65, s64, 32768\n"
      "s_cmp_eq_u32 s40, 0\n"
      "s_cselect_b64 s[66:67], -1, 0\n"
      "v_lshlrev_b32 v12, 2, v11\n"
      "v_sub_u32 v12, v7, v12\n"
      "v_mov_b32 v132, 0xffff\n"
      "v_mov_b32 v133, 0xffff0000\n"
      "v_subrev_u32 v128, 0, v12\n"
      "v_ashrrev_i32 v128, 3, v128\n"
      "v_cmp_gt_i32 vcc, 0, v128\n"
      "s_nop 0\n"
      "s_and_b64 s[56:57], vcc, s[66:67]\n"
      "v_subrev_u32 v129, 1, v12\n"
      "v_ashrrev_i32 v129, 3, v129\n"
      "v_cmp_gt_i32 vcc, 0, v129\n"
      "s_nop 0\n"
      "s_and_b64 s[58:59], vcc, s[66:67]\n"
      "v_subrev_u32 v130, 2, v12\n"
      "v_ashrrev_i32 v130, 3, v130\n"
      "v_cmp_gt_i32 vcc, 0, v130\n"
      "s_nop 0\n"
      "s_and_b64 s[60:61], vcc, s[66:67]\n"
      "v_subrev_u32 v131, 3, v12\n"
      "v_ashrrev_i32 v131, 3, v131\n"
      "v_cmp_gt_i32 vcc, 0, v131\n"
      "s_nop 0\n"
      "s_and_b64 s[62:63], vcc, s[66:67]\n"
      "v_cmp_eq_i32 vcc, -1, v128\n"
      "v_cmp_eq_i32 s[68:69], -1, v129\n"
      "s_nop 0\n"
      "v_cndmask_b32 v232, 0, v132, vcc\n"
      "v_cndmask_b32 v13, 0, v133, s[68:69]\n"
      "v_or_b32 v232, v232, v13\n"
      "v_cmp_eq_i32 vcc, -1, v130\n"
      "v_cmp_eq_i32 s[68:69], -1, v131\n"
      "s_nop 0\n"
      "v_cndmask_b32 v237, 0, v132, vcc\n"
      "v_cndmask_b32 v13, 0, v133, s[68:69]\n"
      "v_or_b32 v237, v237, v13\n"
      "v_cmp_eq_i32 vcc, 0, v128\n"
      "v_cmp_eq_i32 s[68:69], 0, v129\n"
      "s_nop 0\n"
      "v_cndmask_b32 v233, 0, v132, vcc\n"
      "v_cndmask_b32 v13, 0, v133, s[68:69]\n"
      "v_or_b32 v233, v233, v13\n"
      "v_cmp_eq_i32 vcc, 0, v130\n"
      "v_cmp_eq_i32 s[68:69], 0, v131\n"
      "s_nop 0\n"
      "v_cndmask_b32 v238, 0, v132, vcc\n"
      "v_cndmask_b32 v13, 0, v133, s[68:69]\n"
      "v_or_b32 v238, v238, v13\n"
      "v_cmp_eq_i32 vcc, 1, v128\n"
      "v_cmp_eq_i32 s[68:69], 1, v129\n"
      "s_nop 0\n"
      "v_cndmask_b32 v234, 0, v132, vcc\n"
      "v_cndmask_b32 v13, 0, v133, s[68:69]\n"
      "v_or_b32 v234, v234, v13\n"
      "v_cmp_eq_i32 vcc, 1, v130\n"
      "v_cmp_eq_i32 s[68:69], 1, v131\n"
      "s_nop 0\n"
      "v_cndmask_b32 v239, 0, v132, vcc\n"
      "v_cndmask_b32 v13, 0, v133, s[68:69]\n"
      "v_or_b32 v239, v239, v13\n"
      "v_cmp_eq_i32 vcc, 2, v128\n"
      "v_cmp_eq_i32 s[68:69], 2, v129\n"
      "s_nop 0\n"
      "v_cndmask_b32 v235, 0, v132, vcc\n"
      "v_cndmask_b32 v13, 0, v133, s[68:69]\n"
      "v_or_b32 v235, v235, v13\n"
      "v_cmp_eq_i32 vcc, 2, v130\n"
      "v_cmp_eq_i32 s[68:69], 2, v131\n"
      "s_nop 0\n"
      "v_cndmask_b32 v240, 0, v132, vcc\n"
      "v_cndmask_b32 v13, 0, v133, s[68:69]\n"
      "v_or_b32 v240, v240, v13\n"
      "v_cmp_eq_i32 vcc, 3, v128\n"
      "v_cmp_eq_i32 s[68:69], 3, v129\n"
      "s_nop 0\n"
      "v_cndmask_b32 v236, 0, v132, vcc\n"
      "v_cndmask_b32 v13, 0, v133, s[68:69]\n"
      "v_or_b32 v236, v236, v13\n"
      "v_cmp_eq_i32 vcc, 3, v130\n"
      "v_cmp_eq_i32 s[68:69], 3, v131\n"
      "s_nop 0\n"
      "v_cndmask_b32 v241, 0, v132, vcc\n"
      "v_cndmask_b32 v13, 0, v133, s[68:69]\n"
      "v_or_b32 v241, v241, v13\n"
      "v_mov_b32 v182, 0\n"
      "v_mov_b32 v183, 0\n"
      "v_mov_b32 v190, 0\n"
      "v_mov_b32 v191, 0\n"
      "v_mov_b32 v198, 0\n"
      "v_mov_b32 v199, 0\n"
      "v_mov_b32 v206, 0\n"
      "v_mov_b32 v207, 0\n"
      "v_mov_b32 v230, 0\n"
      "v_mov_b32 v231, 0\n"
      "v_and_b32 v12, 15, v1\n"
      "s_cmp_eq_u32 s38, 0\n"
      "s_cselect_b32 s40, 7, 15\n"
      "v_and_b32 v12, s40, v12\n"
      "v_lshlrev_b32 v12, 4, v12\n"
      "v_lshl_add_u32 v12, v11, 8, v12\n"
      "s_lshl_b32 s40, s39, 10\n"
      "s_add_u32 s40, s40, 155648\n"
      "v_add_u32 v246, s40, v12\n"
      "s_lshl_b32 s40, s39, 11\n"
      "s_add_u32 s40, s40, 157696\n"
      "v_add_u32 v247, s40, v8\n"
      "s_lshl_b32 s40, s3, 2\n"
      "s_add_u32 s40, s40, 161808\n"
      "v_mov_b32 v248, s40\n"
      "v_xor_b32 v249, 4, v248\n"
      "v_mov_b32 v250, 0\n"
      "v_mov_b32 v251, 0\n"
      "s_cmp_eq_u32 s38, 0\n"
      "s_cbranch_scc1 .Lmk_loop3\n"
      ".Lmk_loop4:\n"
      "v_add_u32 v3, s47, v2\n"
      "v_add_u32_e64 v6, s47, v5\n"
      "s_waitcnt lgkmcnt(0)\n"
      "s_barrier\n"
      "v_mfma_f32_32x32x16_bf16 v[16:31], v[164:167], v[152:155], v[16:31]\n"
      "ds_read_b128 v[128:131], v3 offset:0\n"
      "ds_read_b64_tr_b16 v[140:141], v6 offset:0\n"
      "v_mfma_f32_32x32x16_bf16 v[32:47], v[164:167], v[156:159], v[32:47]\n"
      "ds_read_b64_tr_b16 v[142:143], v6 offset:336\n"
      "ds_read_b128 v[132:135], v3 offset:4096\n"
      "v_mfma_f32_32x32x16_bf16 v[48:63], v[168:171], v[152:155], v[48:63]\n"
      "ds_read_b64_tr_b16 v[144:145], v6 offset:112\n"
      "ds_read_b64_tr_b16 v[146:147], v6 offset:448\n"
      "v_mfma_f32_32x32x16_bf16 v[64:79], v[168:171], v[156:159], v[64:79]\n"
      "ds_read_b64_tr_b16 v[148:149], v6 offset:224\n"
      "ds_read_b64_tr_b16 v[150:151], v6 offset:560\n"
      "v_mfma_f32_32x32x16_bf16 v[80:95], v[152:155], v[172:175], v[80:95]\n"
      "ds_read_b128 v[136:139], v3 offset:8192\n"
      "v_mfma_f32_32x32x16_bf16 v[96:111], v[156:159], v[172:175], v[96:111]\n"
      "v_mfma_f32_32x32x16_bf16 v[112:127], v[160:163], v[172:175], v[112:127]\n"
      ".Lmk_pad3:\n"
      "s_cbranch_execz .Lmk_padr3\n"
      "s_nop 0\n"
      "v_xor_b32 v4, 32, v3\n"
      "s_waitcnt lgkmcnt(6)\n"
      "v_mfma_f32_32x32x16_bf16 v[16:31], v[140:143], v[128:131], v[16:31]\n"
      "ds_read_b128 v[152:155], v4 offset:0\n"
      "ds_read_b64_tr_b16 v[164:165], v6 offset:5376\n"
      "s_waitcnt lgkmcnt(7)\n"
      "v_mfma_f32_32x32x16_bf16 v[32:47], v[140:143], v[132:135], v[32:47]\n"
      "ds_read_b64_tr_b16 v[166:167], v6 offset:5712\n"
      "ds_read_b128 v[156:159], v4 offset:4096\n"
      "s_waitcnt lgkmcnt(7)\n"
      "v_mfma_f32_32x32x16_bf16 v[48:63], v[144:147], v[128:131], v[48:63]\n"
      "ds_read_b64_tr_b16 v[168:169], v6 offset:5488\n"
      "ds_read_b64_tr_b16 v[170:171], v6 offset:5824\n"
      "v_mfma_f32_32x32x16_bf16 v[64:79], v[144:147], v[132:135], v[64:79]\n"
      "ds_read_b64_tr_b16 v[172:173], v6 offset:5600\n"
      "ds_read_b64_tr_b16 v[174:175], v6 offset:5936\n"
      "s_waitcnt lgkmcnt(9)\n"
      "v_mfma_f32_32x32x16_bf16 v[80:95], v[128:131], v[148:151], v[80:95]\n"
      "ds_read_b128 v[160:163], v4 offset:8192\n"
      "v_mfma_f32_32x32x16_bf16 v[96:111], v[132:135], v[148:151], v[96:111]\n"
      "s_waitcnt lgkmcnt(9)\n"
      "v_mfma_f32_32x32x16_bf16 v[112:127], v[136:139], v[148:151], v[112:127]\n"
      "v_xor_b32 v4, 64, v3\n"
      "s_waitcnt lgkmcnt(6)\n"
      "v_mfma_f32_32x32x16_bf16 v[16:31], v[164:167], v[152:155], v[16:31]\n"
      "ds_read_b128 v[128:131], v4 offset:0\n"
      "ds_read_b64_tr_b16 v[140:141], v6 offset:10752\n"
      "s_waitcnt lgkmcnt(7)\n"
      "v_mfma_f32_32x32x16_bf16 v[32:47], v[164:167], v[156:159], v[32:47]\n"
      "ds_read_b64_tr_b16 v[142:143], v6 offset:11088\n"
      "ds_read_b128 v[132:135], v4 offset:4096\n"
      "s_waitcnt lgkmcnt(7)\n"
      "v_mfma_f32_32x32x16_bf16 v[48:63], v[168:171], v[152:155], v[48:63]\n"
      "ds_read_b64_tr_b16 v[144:145], v6 offset:10864\n"
      "ds_read_b64_tr_b16 v[146:147], v6 offset:11200\n"
      "v_mfma_f32_32x32x16_bf16 v[64:79], v[168:171], v[156:159], v[64:79]\n"
      "ds_read_b64_tr_b16 v[148:149], v6 offset:10976\n"
      "ds_read_b64_tr_b16 v[150:151], v6 offset:11312\n"
      "s_waitcnt lgkmcnt(9)\n"
      "v_mfma_f32_32x32x16_bf16 v[80:95], v[152:155], v[172:175], v[80:95]\n"
      "ds_read_b128 v[136:139], v4 offset:8192\n"
      "v_mfma_f32_32x32x16_bf16 v[96:111], v[156:159], v[172:175], v[96:111]\n"
      "s_waitcnt lgkmcnt(9)\n"
      "v_mfma_f32_32x32x16_bf16 v[112:127], v[160:163], v[172:175], v[112:127]\n"
      "v_xor_b32 v4, 96, v3\n"
      "s_waitcnt lgkmcnt(6)\n"
      "v_mfma_f32_32x32x16_bf16 v[16:31], v[140:143], v[128:131], v[16:31]\n"
      "ds_read_b128 v[152:155], v4 offset:0\n"
      "ds_read_b64_tr_b16 v[164:165], v6 offset:16128\n"
      "s_waitcnt lgkmcnt(7)\n"
      "v_mfma_f32_32x32x16_bf16 v[32:47], v[140:143], v[132:135], v[32:47]\n"
      "ds_read_b64_tr_b16 v[166:167], v6 offset:16464\n"
      "ds_read_b128 v[156:159], v4 offset:4096\n"
      "s_waitcnt lgkmcnt(7)\n"
      "s_nop 0\n"
      "v_mfma_f32_32x32x16_bf16 v[48:63], v[144:147], v[128:131], v[48:63]\n"
      "ds_read_b64_tr_b16 v[168:169], v6 offset:16240\n"
      "ds_read_b64_tr_b16 v[170:171], v6 offset:16576\n"
      "v_mfma_f32_32x32x16_bf16 v[64:79], v[144:147], v[132:135], v[64:79]\n"
      "ds_read_b64_tr_b16 v[172:173], v6 offset:16352\n"
      "ds_read_b64_tr_b16 v[174:175], v6 offset:16688\n"
      "s_waitcnt lgkmcnt(9)\n"
      "v_mfma_f32_32x32x16_bf16 v[80:95], v[128:131], v[148:151], v[80:95]\n"
      "ds_read_b128 v[160:163], v4 offset:8192\n"
      "s_add_u32 s47, s47, 39936\n"
      "s_cmp_eq_u32 s47, 119808\n"
      "s_cselect_b32 s47, 0, s47\n"
      "s_sub_u32 s52, s52, 1\n"
      "v_mfma_f32_32x32x16_bf16 v[96:111], v[132:135], v[148:151], v[96:111]\n"
      "s_waitcnt lgkmcnt(9)\n"
      "v_mfma_f32_32x32x16_bf16 v[112:127], v[136:139], v[148:151], v[112:127]\n"
      "s_cbranch_scc0 .Lmk_loop4\n"
      "s_waitcnt lgkmcnt(0)\n"
      "v_mfma_f32_32x32x16_bf16 v[48:63], v[168:171], v[152:155], v[48:63]\n"
      "v_mfma_f32_32x32x16_bf16 v[64:79], v[168:171], v[156:159], v[64:79]\n"
      "v_mfma_f32_32x32x16_bf16 v[16:31], v[164:167], v[152:155], v[16:31]\n"
      "v_mfma_f32_32x32x16_bf16 v[32:47], v[164:167], v[156:159], v[32:47]\n"
      "v_mfma_f32_32x32x16_bf16 v[80:95], v[152:155], v[172:175], v[80:95]\n"
      "v_mfma_f32_32x32x16_bf16 v[96:111], v[156:159], v[172:175], v[96:111]\n"
      "v_mfma_f32_32x32x16_bf16 v[112:127], v[160:163], v[172:175], v[112:127]\n"
      "s_branch .Lmk_epiA\n"
      ".Lmk_loop3:\n"
      "v_add_u32 v3, s47, v2\n"
      "v_add_u32_e64 v6, s47, v5\n"
      "s_waitcnt lgkmcnt(0)\n"
      "s_barrier\n"
      "v_mfma_f32_32x32x16_bf16 v[16:31], v[164:167], v[152:155], v[16:31]\n"
      "ds_read_b128 v[128:131], v3 offset:0\n"
      "ds_read_b64_tr_b16 v[140:141], v6 offset:0\n"
      "v_mfma_f32_32x32x16_bf16 v[32:47], v[164:167], v[156:159], v[32:47]\n"
      "ds_read_b64_tr_b16 v[142:143], v6 offset:336\n"
      "ds_read_b128 v[132:135], v3 offset:4096\n"
      "v_mfma_f32_32x32x16_bf16 v[48:63], v[168:171], v[152:155], v[48:63]\n"
      "ds_read_b64_tr_b16 v[144:145], v6 offset:112\n"
      "ds_read_b64_tr_b16 v[146:147], v6 offset:448\n"
      "v_mfma_f32_32x32x16_bf16 v[64:79], v[168:171], v[156:159], v[64:79]\n"
      "ds_read_b64_tr_b16 v[148:149], v6 offset:224\n"
      "ds_read_b64_tr_b16 v[150:151], v6 offset:560\n"
      "v_mfma_f32_32x32x16_bf16 v[80:95], v[152:155], v[172:175], v[80:95]\n"
      "ds_read_b128 v[136:139], v3 offset:16384\n"
      "v_mfma_f32_32x32x16_bf16 v[96:111], v[156:159], v[172:175], v[96:111]\n"
      "v_mfma_f32_32x32x16_bf16 v[112:127], v[168:171], v[160:163], v[112:127]\n"
      "v_xor_b32 v4, 32, v3\n"
      "s_waitcnt lgkmcnt(6)\n"
      "v_mfma_f32_32x32x16_bf16 v[16:31], v[140:143], v[128:131], v[16:31]\n"
      "ds_read_b128 v[152:155], v4 offset:0\n"
      "ds_read_b64_tr_b16 v[164:165], v6 offset:5376\n"
      "s_waitcnt lgkmcnt(7)\n"
      "v_mfma_f32_32x32x16_bf16 v[32:47], v[140:143], v[132:135], v[32:47]\n"
      "ds_read_b64_tr_b16 v[166:167], v6 offset:5712\n"
      "ds_read_b128 v[156:159], v4 offset:4096\n"
      "s_waitcnt lgkmcnt(7)\n"
      "v_mfma_f32_32x32x16_bf16 v[48:63], v[144:147], v[128:131], v[48:63]\n"
      "ds_read_b64_tr_b16 v[168:169], v6 offset:5488\n"
      "ds_read_b64_tr_b16 v[170:171], v6 offset:5824\n"
      "v_mfma_f32_32x32x16_bf16 v[64:79], v[144:147], v[132:135], v[64:79]\n"
      "ds_read_b64_tr_b16 v[172:173], v6 offset:5600\n"
      "ds_read_b64_tr_b16 v[174:175], v6 offset:5936\n"
      "s_waitcnt lgkmcnt(9)\n"
      "v_mfma_f32_32x32x16_bf16 v[80:95], v[128:131], v[148:151], v[80:95]\n"
      "ds_read_b128 v[160:163], v4 offset:16384\n"
      "v_mfma_f32_32x32x16_bf16 v[96:111], v[132:135], v[148:151], v[96:111]\n"
      "s_waitcnt lgkmcnt(9)\n"
      "v_mfma_f32_32x32x16_bf16 v[112:127], v[144:147], v[136:139], v[112:127]\n"
      "v_xor_b32 v4, 64, v3\n"
      "s_waitcnt lgkmcnt(6)\n"
      "v_mfma_f32_32x32x16_bf16 v[16:31], v[164:167], v[152:155], v[16:31]\n"
      "ds_read_b128 v[128:131], v4 offset:0\n"
      "ds_read_b64_tr_b16 v[140:141], v6 offset:10752\n"
      "s_waitcnt lgkmcnt(7)\n"
      "v_mfma_f32_32x32x16_bf16 v[32:47], v[164:167], v[156:159], v[32:47]\n"
      "ds_read_b64_tr_b16 v[142:143], v6 offset:11088\n"
      "ds_read_b128 v[132:135], v4 offset:4096\n"
      "s_waitcnt lgkmcnt(7)\n"
      "v_mfma_f32_32x32x16_bf16 v[48:63], v[168:171], v[152:155], v[48:63]\n"
      "ds_read_b64_tr_b16 v[144:145], v6 offset:10864\n"
      "ds_read_b64_tr_b16 v[146:147], v6 offset:11200\n"
      "v_mfma_f32_32x32x16_bf16 v[64:79], v[168:171], v[156:159], v[64:79]\n"
      "ds_read_b64_tr_b16 v[148:149], v6 offset:10976\n"
      "ds_read_b64_tr_b16 v[150:151], v6 offset:11312\n"
      "s_waitcnt lgkmcnt(9)\n"
      "v_mfma_f32_32x32x16_bf16 v[80:95], v[152:155], v[172:175], v[80:95]\n"
      "ds_read_b128 v[136:139], v4 offset:16384\n"
      "v_mfma_f32_32x32x16_bf16 v[96:111], v[156:159], v[172:175], v[96:111]\n"
      "s_waitcnt lgkmcnt(9)\n"
      "v_mfma_f32_32x32x16_bf16 v[112:127], v[168:171], v[160:163], v[112:127]\n"
      "v_xor_b32 v4, 96, v3\n"
      "s_waitcnt lgkmcnt(6)\n"
      "v_mfma_f32_32x32x16_bf16 v[16:31], v[140:143], v[128:131], v[16:31]\n"
      "ds_read_b128 v[152:155], v4 offset:0\n"
      "ds_read_b64_tr_b16 v[164:165], v6 offset:16128\n"
      "s_waitcnt lgkmcnt(7)\n"
      "v_mfma_f32_32x32x16_bf16 v[32:47], v[140:143], v[132:135], v[32:47]\n"
      "ds_read_b64_tr_b16 v[166:167], v6 offset:16464\n"
      "ds_read_b128 v[156:159], v4 offset:4096\n"
      "s_waitcnt lgkmcnt(7)\n"
      "s_nop 0\n"
      "v_mfma_f32_32x32x16_bf16 v[48:63], v[144:147], v[128:131], v[48:63]\n"
      "ds_read_b64_tr_b16 v[168:169], v6 offset:16240\n"
      "ds_read_b64_tr_b16 v[170:171], v6 offset:16576\n"
      "v_mfma_f32_32x32x16_bf16 v[64:79], v[144:147], v[132:135], v[64:79]\n"
      "ds_read_b64_tr_b16 v[172:173], v6 offset:16352\n"
      "ds_read_b64_tr_b16 v[174:175], v6 offset:16688\n"
      "s_waitcnt lgkmcnt(9)\n"
      "v_mfma_f32_32x32x16_bf16 v[80:95], v[128:131], v[148:151], v[80:95]\n"
      "ds_read_b128 v[160:163], v4 offset:16384\n"
      "s_add_u32 s47, s47, 39936\n"
      "s_cmp_eq_u32 s47, 119808\n"
      "s_cselect_b32 s47, 0, s47\n"
      "s_sub_u32 s52, s52, 1\n"
      "v_mfma_f32_32x32x16_bf16 v[96:111], v[132:135], v[148:151], v[96:111]\n"
      "s_waitcnt lgkmcnt(9)\n"
      "v_mfma_f32_32x32x16_bf16 v[112:127], v[144:147], v[136:139], v[112:127]\n"
      "s_cbranch_scc0 .Lmk_loop3\n"
      "s_waitcnt lgkmcnt(0)\n"
      "v_mfma_f32_32x32x16_bf16 v[48:63], v[168:171], v[152:155], v[48:63]\n"
      "v_mfma_f32_32x32x16_bf16 v[64:79], v[168:171], v[156:159], v[64:79]\n"
      "v_mfma_f32_32x32x16_bf16 v[16:31], v[164:167], v[152:155], v[16:31]\n"
      "v_mfma_f32_32x32x16_bf16 v[32:47], v[164:167], v[156:159], v[32:47]\n"
      "v_mfma_f32_32x32x16_bf16 v[80:95], v[152:155], v[172:175], v[80:95]\n"
      "v_mfma_f32_32x32x16_bf16 v[96:111], v[156:159], v[172:175], v[96:111]\n"
      "v_mfma_f32_32x32x16_bf16 v[112:127], v[168:171], v[160:163], v[112:127]\n"
      ".Lmk_epiA:\n"
      "s_add_u32 s28, s28, 0x7F4A7C15\n"
      "s_addc_u32 s29, s29, 0x9E3779B9\n"
      "v_mov_b32 v12, s28\n"
      "v_mov_b32_e64 v13, s29\n"
      "s_nop 1\n"
      "v_cvt_pk_bf16_f32 v192, v48, v49\n"
      "v_cvt_pk_bf16_f32 v193, v50, v51\n"
      "v_cvt_pk_bf16_f32 v194, v52, v53\n"
      "v_cvt_pk_bf16_f32 v195, v54, v55\n"
      "v_cvt_pk_bf16_f32 v196, v56, v57\n"
      "v_cvt_pk_bf16_f32 v197, v58, v59\n"
      "v_cvt_pk_bf16_f32 v200, v64, v65\n"
      "v_cvt_pk_bf16_f32 v201, v66, v67\n"
      "v_cvt_pk_bf16_f32 v202, v68, v69\n"
      "v_cvt_pk_bf16_f32 v203, v70, v71\n"
      "v_cvt_pk_bf16_f32 v204, v72, v73\n"
      "v_cvt_pk_bf16_f32 v205, v74, v75\n"
      "v_cvt_pk_bf16_f32 v176, v16, v17\n"
      "v_cvt_pk_bf16_f32 v177, v18, v19\n"
      "v_cvt_pk_bf16_f32 v178, v20, v21\n"
      "v_cvt_pk_bf16_f32 v179, v22, v23\n"
      "v_cvt_pk_bf16_f32 v180, v24, v25\n"
      "v_cvt_pk_bf16_f32 v181, v26, v27\n"
      "v_cvt_pk_bf16_f32 v184, v32, v33\n"
      "v_cvt_pk_bf16_f32 v185, v34, v35\n"
      "v_cvt_pk_bf16_f32 v186, v36, v37\n"
      "v_cvt_pk_bf16_f32 v187, v38, v39\n"
      "v_cvt_pk_bf16_f32 v188, v40, v41\n"
      "v_cvt_pk_bf16_f32 v189, v42, v43\n"
      "s_mov_b64 exec, s[82:83]\n"
      "s_nop 0\n"
      "v_cvt_pk_bf16_f32 v198, v60, v61\n"
      "v_cvt_pk_bf16_f32 v199, v62, v63\n"
      "v_cvt_pk_bf16_f32 v206, v76, v77\n"
      "v_cvt_pk_bf16_f32 v207, v78, v79\n"
      "v_cvt_pk_bf16_f32 v182, v28, v29\n"
      "v_cvt_pk_bf16_f32 v183, v30, v31\n"
      "v_cvt_pk_bf16_f32 v190, v44, v45\n"
      "v_cvt_pk_bf16_f32 v191, v46, v47\n"
      "v_cvt_pk_bf16_f32 v230, v124, v125\n"
      "v_cvt_pk_bf16_f32 v231, v126, v127\n"
      "s_mov_b64 exec, -1\n"
      "s_nop 0\n"
      "v_cvt_pk_bf16_f32 v224, v112, v113\n"
      "v_cvt_pk_bf16_f32 v225, v114, v115\n"
      "v_cvt_pk_bf16_f32 v226, v116, v117\n"
      "v_cvt_pk_bf16_f32 v227, v118, v119\n"
      "v_cvt_pk_bf16_f32 v228, v120, v121\n"
      "v_cvt_pk_bf16_f32 v229, v122, v123\n"
      "v_mfma_f32_32x32x16_bf16 v[16:31], v[192:195], v[176:179], 0\n"
      "v_cvt_pk_bf16_f32 v208, v80, v81\n"
      "v_cvt_pk_bf16_f32 v209, v82, v83\n"
      "v_cvt_pk_bf16_f32 v210, v84, v85\n"
      "v_cvt_pk_bf16_f32 v211, v86, v87\n"
      "v_cvt_pk_bf16_f32 v212, v88, v89\n"
      "v_cvt_pk_bf16_f32 v213, v90, v91\n"
      "v_cvt_pk_bf16_f32 v214, v92, v93\n"
      "v_cvt_pk_bf16_f32 v215, v94, v95\n"
      "v_mfma_f32_32x32x16_bf16 v[32:47], v[200:203], v[184:187], 0\n"
      "v_cvt_pk_bf16_f32 v216, v96, v97\n"
      "v_cvt_pk_bf16_f32 v217, v98, v99\n"
      "v_cvt_pk_bf16_f32 v218, v100, v101\n"
      "v_cvt_pk_bf16_f32 v219, v102, v103\n"
      "v_cvt_pk_bf16_f32 v220, v104, v105\n"
      "v_cvt_pk_bf16_f32 v221, v106, v107\n"
      "v_cvt_pk_bf16_f32 v222, v108, v109\n"
      "v_cvt_pk_bf16_f32 v223, v110, v111\n"
      "v_mfma_f32_32x32x16_bf16 v[16:31], v[196:199], v[180:183], v[16:31]\n"
      "s_cmp_eq_u32 s38, 0\n"
      "s_cbranch_scc1 .Lmk_ex0\n"
      "ds_write_b128 v247, v[224:227]\n"
      "v_mfma_f32_32x32x16_bf16 v[32:47], v[204:207], v[188:191], v[32:47]\n"
      "ds_write_b32 v248, v248\n"
      "s_mov_b32 s95, 1048576\n"
      ".Lmk_pair33:\n"
      "ds_read_b32 v14, v249\n"
      "s_waitcnt lgkmcnt(0)\n"
      "v_cmp_eq_u32 vcc, 0, v14\n"
      "s_cbranch_vccz .Lmk_paird34\n"
      "s_sub_u32 s95, s95, 1\n"
      "s_cbranch_scc0 .Lmk_pair33\n"
      ".Lmk_paird34:\n"
      "ds_read_b128 v[224:227], v246\n"
      "ds_read_b128 v[228:231], v246 offset:512\n"
      "ds_read_b128 v[242:245], v247 offset:1024\n"
      "s_waitcnt lgkmcnt(1)\n"
      "s_branch .Lmk_ex1\n"
      ".Lmk_ex0:\n"
      "s_nop 0\n"
      "ds_write_b64 v247, v[214:215] offset:1024\n"
      "ds_write_b64 v247, v[222:223] offset:1032\n"
      "s_mov_b32 exec_lo, 0xff000000\n"
      "s_mov_b32 exec_hi, 0xff000000\n"
      "ds_write_b128 v246, v[192:195] offset:0\n"
      "ds_write_b128 v246, v[196:199] offset:512\n"
      "ds_write_b128 v246, v[200:203] offset:128\n"
      "ds_write_b128 v246, v[204:207] offset:640\n"
      "s_mov_b64 exec, -1\n"
      "v_mfma_f32_32x32x16_bf16 v[32:47], v[204:207], v[188:191], v[32:47]\n"
      "ds_write_b32 v248, v248\n"
      "s_mov_b32 s95, 1048576\n"
      ".Lmk_pair35:\n"
      "ds_read_b32 v14, v249\n"
      "s_waitcnt lgkmcnt(0)\n"
      "v_cmp_eq_u32 vcc, 0, v14\n"
      "s_cbranch_vccz .Lmk_paird36\n"
      "s_sub_u32 s95, s95, 1\n"
      "s_cbranch_scc0 .Lmk_pair35\n"
      ".Lmk_paird36:\n"
      "ds_read_b128 v[242:245], v247\n"
      ".Lmk_ex1:\n"
      "v_mfma_f32_32x32x16_bf16 v[48:63], v[224:227], v[176:179], 0\n"
      "v_mfma_f32_32x32x16_bf16 v[64:79], v[224:227], v[184:187], 0\n"
      "v_mfma_f32_32x32x16_bf16 v[48:63], v[228:231], v[180:183], v[48:63]\n"
      "v_mfma_f32_32x32x16_bf16 v[64:79], v[228:231], v[188:191], v[64:79]\n"
      "v_cvt_pk_bf16_f32 v130, v16, v17\n"
      "v_cvt_pk_bf16_f32 v146, v32, v33\n"
      "v_and_b32 v128, v130, v233\n"
      "v_and_b32 v144, v146, v233\n"
      "v_cvt_pk_bf16_f32 v130, v18, v19\n"
      "v_cvt_pk_bf16_f32 v146, v34, v35\n"
      "v_and_b32 v129, v130, v238\n"
      "v_and_b32 v145, v146, v238\n"
      "v_cvt_pk_bf16_f32 v130, v20, v21\n"
      "v_cvt_pk_bf16_f32 v146, v36, v37\n"
      "v_and_or_b32 v128, v130, v234, v128\n"
      "v_and_or_b32 v144, v146, v234, v144\n"
      "v_cvt_pk_bf16_f32 v130, v22, v23\n"
      "v_cvt_pk_bf16_f32 v146, v38, v39\n"
      "v_and_or_b32 v129, v130, v239, v129\n"
      "v_and_or_b32 v145, v146, v239, v145\n"
      "v_cvt_pk_bf16_f32 v130, v24, v25\n"
      "v_cvt_pk_bf16_f32 v146, v40, v41\n"
      "v_and_or_b32 v128, v130, v235, v128\n"
      "v_and_or_b32 v144, v146, v235, v144\n"
      "v_cvt_pk_bf16_f32 v130, v26, v27\n"
      "v_cvt_pk_bf16_f32 v146, v42, v43\n"
      "v_and_or_b32 v129, v130, v240, v129\n"
      "v_and_or_b32 v145, v146, v240, v145\n"
      "v_cvt_pk_bf16_f32 v130, v28, v29\n"
      "v_cvt_pk_bf16_f32 v146, v44, v45\n"
      "v_and_or_b32 v128, v130, v236, v128\n"
      "v_and_or_b32 v144, v146, v236, v144\n"
      "v_cvt_pk_bf16_f32 v130, v30, v31\n"
      "v_cvt_pk_bf16_f32 v146, v46, v47\n"
      "v_and_or_b32 v129, v130, v241, v129\n"
      "v_and_or_b32 v145, v146, v241, v145\n"
      "v_cvt_pk_bf16_f32 v130, v48, v49\n"
      "v_cvt_pk_bf16_f32 v146, v68, v69\n"
      "v_and_or_b32 v128, v130, v232, v128\n"
      "v_and_or_b32 v144, v146, v232, v144\n"
      "v_cvt_pk_bf16_f32 v130, v50, v51\n"
      "v_cvt_pk_bf16_f32 v146, v70, v71\n"
      "v_and_or_b32 v129, v130, v237, v129\n"
      "v_and_or_b32 v145, v146, v237, v145\n"
      "s_mov_b32 s40, 0x3e41848f\n"
      "s_mov_b32 s41, 0xffff0000\n"
      "s_mov_b32 s68, 0x3fb8aa3b\n"
      "v_lshlrev_b32 v132, 16, v128\n"
      "v_and_b32 v133, s41, v128\n"
      "v_lshlrev_b32 v134, 16, v129\n"
      "v_and_b32 v135, s41, v129\n"
      "v_lshlrev_b32 v148, 16, v144\n"
      "v_and_b32 v149, s41, v144\n"
      "v_lshlrev_b32 v150, 16, v145\n"
      "v_and_b32 v151, s41, v145\n"
      "v_mul_f32 v132, s40, v132\n"
      "v_mul_f32 v148, s40, v148\n"
      "v_mul_f32 v133, s40, v133\n"
      "v_mul_f32 v149, s40, v149\n"
      "v_mul_f32 v134, s40, v134\n"
      "v_mul_f32 v150, s40, v150\n"
      "v_mul_f32 v135, s40, v135\n"
      "v_mul_f32 v151, s40, v151\n"
      "v_cvt_pk_bf16_f32 v128, v132, v133\n"
      "v_cvt_pk_bf16_f32 v129, v134, v135\n"
      "v_cvt_pk_bf16_f32 v144, v148, v149\n"
      "v_cvt_pk_bf16_f32 v145, v150, v151\n"
      "v_lshlrev_b32 v132, 16, v128\n"
      "v_and_b32 v133, s41, v128\n"
      "v_lshlrev_b32 v134, 16, v129\n"
      "v_and_b32 v135, s41, v129\n"
      "v_lshlrev_b32 v148, 16, v144\n"
      "v_and_b32 v149, s41, v144\n"
      "v_lshlrev_b32 v150, 16, v145\n"
      "v_and_b32 v151, s41, v145\n"
      "s_cmp_lg_u32 s66, 0\n"
      "s_cbranch_scc1 .Lmk_edge\n"
      ".Lmk_edger:\n"
      "v_max3_f32 v136, v132, v133, v134\n"
      "v_max3_f32 v152, v148, v149, v150\n"
      "v_max_f32 v136, v136, v135\n"
      "v_max_f32 v152, v152, v151\n"
      "s_nop 1\n"
      "v_permlane32_swap_b32 v136, v152\n"
      "v_max_f32 v136, v136, v152\n"
      "v_mul_f32 v152, s68, v136\n"
      "v_mul_f32_e64 v136, s68, v136\n"
      "s_nop 1\n"
      "v_permlane32_swap_b32 v136, v152\n"
      "v_fma_f32 v132, v132, s68, -v136\n"
      "v_fma_f32 v148, v148, s68, -v152\n"
      "v_fma_f32 v133, v133, s68, -v136\n"
      "v_fma_f32 v149, v149, s68, -v152\n"
      "v_fma_f32 v134, v134, s68, -v136\n"
      "v_fma_f32 v150, v150, s68, -v152\n"
      "v_fma_f32 v135, v135, s68, -v136\n"
      "v_fma_f32 v151, v151, s68, -v152\n"
      "v_exp_f32 v132, v132\n"
      "v_exp_f32 v148, v148\n"
      "v_exp_f32 v133, v133\n"
      "v_exp_f32 v149, v149\n"
      "v_exp_f32 v134, v134\n"
      "v_exp_f32 v150, v150\n"
      "v_exp_f32 v135, v135\n"
      "v_exp_f32 v151, v151\n"
      "v_add_f32 v136, v132, v133\n"
      "v_add_f32 v152, v148, v149\n"
      "v_add_f32 v137, v134, v135\n"
      "v_add_f32 v153, v150, v151\n"
      "v_add_f32 v136, v136, v137\n"
      "v_add_f32 v152, v152, v153\n"
      "s_nop 1\n"
      "v_permlane32_swap_b32 v136, v152\n"
      "v_add_f32 v136, v136, v152\n"
      "v_rcp_f32 v136, v136\n"
      "s_nop 0\n"
      "v_mov_b32 v152, v136\n"
      "s_nop 1\n"
      "v_permlane32_swap_b32 v136, v152\n"
      "v_mul_f32 v132, v136, v132\n"
      "v_mul_f32 v148, v152, v148\n"
      "v_mul_f32 v133, v136, v133\n"
      "v_mul_f32 v149, v152, v149\n"
      "v_mul_f32 v134, v136, v134\n"
      "v_mul_f32 v150, v152, v150\n"
      "v_mul_f32 v135, v136, v135\n"
      "v_mul_f32 v151, v152, v151\n"
      "v_cvt_pk_bf16_f32 v128, v132, v133\n"
      "v_cvt_pk_bf16_f32 v129, v134, v135\n"
      "v_cvt_pk_bf16_f32 v144, v148, v149\n"
      "v_cvt_pk_bf16_f32 v145, v150, v151\n"
      "v_and_b32 v112, v128, v233\n"
      "v_and_b32 v113, v129, v238\n"
      "v_and_b32 v114, v128, v234\n"
      "v_and_b32 v115, v129, v239\n"
      "v_and_b32 v120, v144, v233\n"
      "s_barrier\n"
      "buffer_load_dwordx4 v[40:43], v8, s[12:15], s85 offen offset:0 sc1\n"
      "buffer_load_dwordx4 v[160:163], v10, s[12:15], s84 offen offset:0 sc1\n"
      "v_and_b32 v121, v145, v238\n"
      "v_and_b32 v122, v144, v234\n"
      "v_and_b32_e64 v123, v145, v239\n"
      "buffer_load_dwordx4 v[44:47], v8, s[12:15], s85 offen offset:1024 sc1\n"
      "v_mfma_f32_32x32x16_bf16 v[80:95], v[208:211], v[112:115], 0\n"
      "v_mfma_f32_32x32x16_bf16 v[96:111], v[216:219], v[120:123], 0\n"
      "v_and_b32 v116, v128, v235\n"
      "buffer_load_dwordx4 v[164:167], v10, s[12:15], s84 offen offset:1024 sc1\n"
      "v_and_b32 v117, v129, v240\n"
      "v_and_b32 v118, v128, v236\n"
      "v_and_b32 v119, v129, v241\n"
      "s_mov_b32 s84, 4096\n"
      "buffer_load_dwordx4 v[176:179], v10, s[12:15], s84 offen offset:0 sc1\n"
      "v_and_b32 v124, v144, v235\n"
      "v_and_b32 v125, v145, v240\n"
      "v_and_b32 v126, v144, v236\n"
      "buffer_load_dwordx4 v[180:183], v10, s[12:15], s84 offen offset:1024 sc1\n"
      "v_and_b32 v127, v145, v241\n"
      "v_and_b32 v248, v128, v232\n"
      "v_and_b32 v249, v129, v237\n"
      "s_mov_b32 s84, 0\n"
      "buffer_load_dwordx4 v[168:171], v10, s[12:15], s84 offen offset:2048 sc1\n"
      "v_and_b32 v252, v144, v232\n"
      "v_and_b32 v253, v145, v237\n"
      "v_mfma_f32_32x32x16_bf16 v[80:95], v[212:215], v[116:119], v[80:95]\n"
      "buffer_load_dwordx4 v[172:175], v10, s[12:15], s84 offen offset:3072 sc1\n"
      "s_waitcnt lgkmcnt(0)\n"
      "v_mfma_f32_32x32x16_bf16 v[80:95], v[242:245], v[248:251], v[80:95]\n"
      "v_mfma_f32_32x32x16_bf16 v[96:111], v[220:223], v[124:127], v[96:111]\n"
      "s_mov_b32 s84, 4096\n"
      "buffer_load_dwordx4 v[184:187], v10, s[12:15], s84 offen offset:2048 sc1\n"
      "v_mfma_f32_32x32x16_bf16 v[96:111], v[242:245], v[250:253], v[96:111]\n"
      "v_mov_b32 v238, 0\n"
      "v_mov_b32 v239, 0\n"
      "buffer_load_dwordx4 v[188:191], v10, s[12:15], s84 offen offset:3072 sc1\n"
      "v_mov_b32 v240, v8\n"
      "s_nop 3\n"
      "v_cvt_pk_bf16_f32 v224, v80, v81\n"
      "v_cvt_pk_bf16_f32 v225, v82, v83\n"
      "s_mov_b32 s84, 8192\n"
      "buffer_load_dwordx4 v[192:195], v10, s[12:15], s84 offen offset:0 sc1\n"
      "v_cvt_pk_bf16_f32 v226, v84, v85\n"
      "v_cvt_pk_bf16_f32 v227, v86, v87\n"
      "v_cvt_pk_bf16_f32 v228, v88, v89\n"
      "buffer_load_dwordx4 v[196:199], v10, s[12:15], s84 offen offset:1024 sc1\n"
      "v_cvt_pk_bf16_f32 v229, v90, v91\n"
      "v_cvt_pk_bf16_f32 v232, v96, v97\n"
      "v_cvt_pk_bf16_f32 v233, v98, v99\n"
      "v_cvt_pk_bf16_f32 v234, v100, v101\n"
      "v_cvt_pk_bf16_f32 v235, v102, v103\n"
      "v_cvt_pk_bf16_f32 v236, v104, v105\n"
      "v_cvt_pk_bf16_f32 v237, v106, v107\n"
      "s_mov_b64 exec, s[82:83]\n"
      "v_cvt_pk_bf16_f32 v230, v92, v93\n"
      "v_cvt_pk_bf16_f32 v231, v94, v95\n"
      "v_cvt_pk_bf16_f32 v238, v108, v109\n"
      "v_cvt_pk_bf16_f32 v239, v110, v111\n"
      "s_mov_b64 exec, -1\n"
      "buffer_load_dwordx4 v[200:203], v10, s[12:15], s84 offen offset:2048 sc1\n"
      "buffer_load_dwordx4 v[204:207], v10, s[12:15], s84 offen offset:3072 sc1\n"
      "s_branch .Lmk_attn2\n"
      ".Lmk_edge:\n"
      "s_nop 0\n"
      "v_mov_b32 v14, 0xce6e6b28\n"
      "v_cndmask_b32 v132, v132, v14, s[56:57]\n"
      "v_cndmask_b32 v148, v148, v14, s[56:57]\n"
      "v_cndmask_b32 v133, v133, v14, s[58:59]\n"
      "v_cndmask_b32 v149, v149, v14, s[58:59]\n"
      "v_cndmask_b32 v134, v134, v14, s[60:61]\n"
      "v_cndmask_b32 v150, v150, v14, s[60:61]\n"
      "v_cndmask_b32 v135, v135, v14, s[62:63]\n"
      "v_cndmask_b32 v151, v151, v14, s[62:63]\n"
      "s_branch .Lmk_edger\n"
      ".Lmk_helper3:\n"
      "s_add_u32 s28, s28, 0x7F4A7C15\n"
      "s_addc_u32 s29, s29, 0x9E3779B9\n"
      "s_mov_b64 s[12:13], s[26:27]\n"
      "s_mov_b32 s14, 11534336\n"
      "s_mov_b32 s15, s7\n"
      "s_mov_b64 s[16:17], s[24:25]\n"
      "s_mov_b32 s18, 3670016\n"
      "s_and_b32 s54, s2, 7\n"
      "s_lshr_b32 s55, s2, 5\n"
      "s_lshl3_add_u32 s40, s54, s55\n"
      "s_bfe_u32 s41, s2, 0x20003\n"
      "s_lshl1_add_u32 s42, s54, s30\n"
      "v_lshlrev_b32 v8, 4, v1\n"
      "v_lshrrev_b32 v12, 4, v1\n"
      "v_and_b32 v13, 15, v1\n"
      "v_lshlrev_b32 v14, 2, v12\n"
      "v_xor_b32 v14, v14, v13\n"
      "v_mul_u32_u24 v15, 0x380, v12\n"
      "v_lshl_add_u32 v14, v14, 4, v15\n"
      "s_lshl_b32 s54, s41, 8\n"
      "v_add_u32 v2, s54, v14\n"
      "s_lshl_b32 s55, s38, 3\n"
      "v_add_u32_e64 v12, s55, v12\n"
      "v_mul_u32_u24 v14, 272, v12\n"
      "v_lshl_add_u32 v14, v13, 4, v14\n"
      "v_add_u32 v7, 139264, v14\n"
      "v_mul_u32_u24 v14, 0x700, v12\n"
      "v_lshl_add_u32 v9, v13, 4, v14\n"
      "s_cmp_eq_u32 s41, 3\n"
      "s_cselect_b32 s56, 8, 16\n"
      "v_cmp_gt_u32 vcc, s56, v13\n"
      "v_mov_b32 v14, 2147483647\n"
      "s_mul_i32 s57, s40, 57344\n"
      "s_add_u32 s57, s57, s54\n"
      "s_add_u32 s57, s57, 896\n"
      "v_cndmask_b32 v9, v14, v9, vcc\n"
      "v_bfe_u32 v12, v1, 2, 2\n"
      "v_xor_b32 v13, s38, v12\n"
      "v_lshlrev_b32 v13, 6, v13\n"
      "v_lshl_add_u32 v13, v12, 8, v13\n"
      "v_and_b32 v12, 16, v1\n"
      "v_lshl_add_u32 v13, v12, 1, v13\n"
      "v_and_b32 v12, 3, v1\n"
      "v_lshl_add_u32 v13, v12, 3, v13\n"
      "v_add_u32 v3, 81920, v13\n"
      "v_lshrrev_b32 v12, 5, v1\n"
      "v_lshl_add_u32 v4, v12, 10, v3\n"
      "v_add_u32 v5, 49152, v8\n"
      "v_subrev_u32 v240, 32768, v3\n"
      "v_subrev_u32 v253, 32768, v4\n"
      "v_and_b32 v13, 31, v1\n"
      "v_mul_u32_u24 v13, 272, v13\n"
      "v_lshl_add_u32 v13, v12, 3, v13\n"
      "s_lshl_b32 s55, s38, 6\n"
      "s_add_u32 s55, s55, 139264\n"
      "v_add_u32 v6, s55, v13\n"
      "v_mov_b32 v10, 161792\n"
      "v_mov_b32 v11, 1\n"
      "s_lshl_b32 s55, s38, 2\n"
      "s_add_u32 s55, s55, 161824\n"
      "v_mov_b32 v252, s55\n"
      "v_and_b32 v12, 7, v1\n"
      "s_lshl_b32 s55, s38, 3\n"
      "v_add_u32 v12, s55, v12\n"
      "v_and_b32 v13, 8, v1\n"
      "v_lshl_add_u32 v12, v13, 6, v12\n"
      "s_lshl_b32 s55, s42, 12\n"
      "s_add_u32 s55, s55, 266240\n"
      "v_lshl_add_u32 v241, v12, 7, s55\n"
      "v_and_b32 v12, 3, v1\n"
      "s_lshr_b32 s55, s40, 1\n"
      "s_lshl2_add_u32 s55, s55, s38\n"
      "s_lshl_b32 s55, s55, 9\n"
      "s_add_u32 s55, s55, 397312\n"
      "v_lshl_add_u32 v242, v12, 7, s55\n"
      "s_lshl2_add_u32 s59, s40, s38\n"
      "s_lshl_b32 s55, s59, 9\n"
      "s_add_u32 s55, s55, 462848\n"
      "v_lshl_add_u32 v243, v12, 7, s55\n"
      "s_lshl_b32 s59, s59, 13\n"
      "s_add_u32 s59, s59, 7340032\n"
      "s_lshl2_add_u32 s60, s31, s38\n"
      "s_lshl_b32 s60, s60, 4\n"
      "s_add_u32 s60, s60, s42\n"
      "s_lshl_b32 s55, s60, 7\n"
      "s_add_u32 s55, s55, 462848\n"
      "v_mov_b32 v244, 2147483647\n"
      "v_writelane_b32 v244, s55, 0\n"
      "s_lshl_b32 s60, s60, 11\n"
      "s_add_u32 s60, s60, 9437184\n"
      "v_mov_b32 v246, s28\n"
      "v_mov_b32 v247, s29\n"
      "v_mov_b32 v248, -1\n"
      "v_mov_b32 v249, -1\n"
      "v_mov_b32 v250, -1\n"
      "v_mov_b32_e64 v251, -1\n"
      "s_mul_i32 s43, s38, 25088\n"
      "s_mul_i32 s61, s38, 7168\n"
      "s_add_u32 s61, s61, 81920\n"
      "s_mov_b32 s45, 0\n"
      "v_subrev_u32 v245, 196608, v241\n"
      "s_lshr_b32 s55, s38, 1\n"
      "s_lshl1_add_u32 s55, s31, s55\n"
      "s_lshl_b32 s56, s42, 5\n"
      "s_add_u32 s55, s55, s56\n"
      "s_lshl_b32 s55, s55, 7\n"
      "s_add_u32 s55, s55, 4096\n"
      "v_writelane_b32 v245, s55, 16\n"
      "s_setprio 3\n"
      "s_mov_b64 exec, 0\n"
      "s_cmp_eq_u32 s38, 0\n"
      "s_cbranch_scc1 .Lmk_pad0\n"
      ".Lmk_padr0:\n"
      "s_cmp_eq_u32 s38, 1\n"
      "s_cbranch_scc1 .Lmk_pad1\n"
      ".Lmk_padr1:\n"
      "s_cmp_eq_u32 s38, 2\n"
      "s_cbranch_scc1 .Lmk_pad2\n"
      ".Lmk_padr2:\n"
      "s_cmp_eq_u32 s38, 3\n"
      "s_cbranch_scc1 .Lmk_pad3\n"
      ".Lmk_padr3:\n"
      "s_cmp_eq_u32 s38, 0\n"
      "s_cbranch_scc1 .Lmk_pad4\n"
      ".Lmk_padr4:\n"
      "s_cmp_eq_u32 s38, 1\n"
      "s_cbranch_scc1 .Lmk_pad5\n"
      ".Lmk_padr5:\n"
      "s_mov_b64 exec, -1\n"
      "s_cmp_lg_u32 s30, 1\n"
      "s_cbranch_scc1 .Lmk_cnd37\n"
      ".Lmk_cnd37:\n"
      "s_cmp_lg_u32 s30, 0\n"
      "s_cbranch_scc1 .Lmk_cnd38\n"
      "s_sleep 40\n"
      ".Lmk_cnd38:\n"
      "s_mov_b32 s95, 131071\n"
      "s_mov_b64 s[86:87], 0x1ffff\n"
      "s_mov_b64 s[88:89], 65536\n"
      ".Lmk_poll39:\n"
      "s_mov_b64 exec, s[88:89]\n"
      "buffer_load_dwordx2 v[14:15], v245, s[12:15], 0 offen sc1\n"
      "s_waitcnt vmcnt(0)\n"
      "v_cmp_ne_u64 vcc, s[28:29], v[14:15]\n"
      "s_andn2_b64 s[86:87], s[86:87], s[88:89]\n"
      "s_or_b64 s[86:87], s[86:87], vcc\n"
      "s_cbranch_scc0 .Lmk_polld40\n"
      "s_ff1_i32_b64 s90, vcc\n"
      "s_lshl_b64 s[88:89], 1, s90\n"
      "s_cmp_eq_u64 vcc, 0\n"
      "s_cselect_b64 s[88:89], s[86:87], s[88:89]\n"
      "s_sub_u32 s95, s95, 1\n"
      "s_cbranch_scc0 .Lmk_poll39\n"
      ".Lmk_polld40:\n"
      "s_mov_b64 exec, -1\n"
      "s_barrier\n"
      ".Lmk_pad4:\n"
      "s_cbranch_execz .Lmk_padr4\n"
      "s_nop 0\n"
      "s_barrier\n"
      "s_add_u32 m0, s61, 0\n"
      "s_add_u32 s62, s43, 0\n"
      "buffer_load_dwordx4 v2, s[4:7], s62 offen offset:0 lds\n"
      "s_add_u32 s62, s62, 2560\n"
      "s_waitcnt vmcnt(3)\n"
      "buffer_load_dwordx4 v2, s[4:7], s62 offen offset:1024 lds\n"
      "s_add_u32 s62, s62, 2560\n"
      "s_waitcnt vmcnt(3)\n"
      "buffer_load_dwordx4 v2, s[4:7], s62 offen offset:2048 lds\n"
      "s_add_u32 s62, s62, 2560\n"
      "s_waitcnt vmcnt(3)\n"
      "buffer_load_dwordx4 v2, s[4:7], s62 offen offset:3072 lds\n"
      "s_add_u32 s62, s62, 2560\n"
      "s_waitcnt vmcnt(3)\n"
      "s_add_u32 m0, m0, 0x1000\n"
      "s_add_u32 s62, s62, 0x1000\n"
      "buffer_load_dwordx4 v2, s[4:7], s62 offen offset:0 lds\n"
      "s_add_u32 s62, s62, 2560\n"
      "s_waitcnt vmcnt(3)\n"
      "buffer_load_dwordx4 v2, s[4:7], s62 offen offset:1024 lds\n"
      "s_add_u32 s62, s62, 2560\n"
      "s_waitcnt vmcnt(3)\n"
      "buffer_load_dwordx4 v2, s[4:7], s62 offen offset:2048 lds\n"
      "s_waitcnt vmcnt(3)\n"
      "s_add_u32 m0, s61, 28672\n"
      "s_add_u32 s62, s43, 100352\n"
      "buffer_load_dwordx4 v2, s[4:7], s62 offen offset:0 lds\n"
      "s_add_u32 s62, s62, 2560\n"
      "s_waitcnt vmcnt(3)\n"
      "buffer_load_dwordx4 v2, s[4:7], s62 offen offset:1024 lds\n"
      "s_add_u32 s62, s62, 2560\n"
      "s_waitcnt vmcnt(3)\n"
      "buffer_load_dwordx4 v2, s[4:7], s62 offen offset:2048 lds\n"
      "s_add_u32 s62, s62, 2560\n"
      "s_waitcnt vmcnt(3)\n"
      "buffer_load_dwordx4 v2, s[4:7], s62 offen offset:3072 lds\n"
      "s_add_u32 s62, s62, 2560\n"
      "s_waitcnt vmcnt(3)\n"
      "s_add_u32 m0, m0, 0x1000\n"
      "s_add_u32 s62, s62, 0x1000\n"
      "buffer_load_dwordx4 v2, s[4:7], s62 offen offset:0 lds\n"
      "s_add_u32 s62, s62, 2560\n"
      "s_waitcnt vmcnt(3)\n"
      "buffer_load_dwordx4 v2, s[4:7], s62 offen offset:1024 lds\n"
      "s_add_u32 s62, s62, 2560\n"
      "s_waitcnt vmcnt(3)\n"
      "buffer_load_dwordx4 v2, s[4:7], s62 offen offset:2048 lds\n"
      "s_waitcnt vmcnt(3)\n"
      "s_waitcnt vmcnt(7) lgkmcnt(0)\n"
      "s_mov_b64 exec, 1\n"
      "ds_add_u32 v10, v11\n"
      "s_mov_b64 exec, -1\n"
      "s_add_u32 s45, s45, 4\n"
      "s_mov_b32 s95, 1048576\n"
      ".Lmk_meet41:\n"
      "ds_read_b32 v12, v10\n"
      "s_waitcnt lgkmcnt(0)\n"
      "v_cmp_gt_u32 vcc, s45, v12\n"
      "s_cbranch_vccz .Lmk_meetd42\n"
      "s_sleep 2\n"
      "s_sub_u32 s95, s95, 1\n"
      "s_cbranch_scc0 .Lmk_meet41\n"
      ".Lmk_meetd42:\n"
      "s_nop 0\n"
      "ds_read_b64_tr_b16 v[16:17], v4 offset:0\n"
      "ds_read_b64_tr_b16 v[18:19], v4 offset:2048\n"
      "ds_read_b64_tr_b16 v[20:21], v4 offset:4096\n"
      "ds_read_b64_tr_b16 v[22:23], v3 offset:6144\n"
      "ds_read_b64_tr_b16 v[24:25], v4 offset:7168\n"
      "ds_read_b64_tr_b16 v[26:27], v4 offset:9216\n"
      "ds_read_b64_tr_b16 v[28:29], v4 offset:11264\n"
      "ds_read_b64_tr_b16 v[30:31], v3 offset:13312\n"
      "ds_read_b64_tr_b16 v[32:33], v4 offset:14336\n"
      "ds_read_b64_tr_b16 v[34:35], v4 offset:16384\n"
      "ds_read_b64_tr_b16 v[36:37], v4 offset:18432\n"
      "ds_read_b64_tr_b16 v[38:39], v3 offset:20480\n"
      "ds_read_b64_tr_b16 v[40:41], v4 offset:21504\n"
      "ds_read_b64_tr_b16 v[42:43], v4 offset:23552\n"
      "ds_read_b64_tr_b16 v[44:45], v4 offset:25600\n"
      "ds_read_b64_tr_b16 v[46:47], v3 offset:27648\n"
      "s_mov_b64 exec, 0xffff\n"
      "buffer_load_dwordx2 v[14:15], v241, s[12:15], 0 offen sc1\n"
      "s_mov_b64 exec, -1\n"
      "s_add_u32 m0, s61, -32768\n"
      "s_add_u32 s62, s43, 200704\n"
      "buffer_load_dwordx4 v2, s[4:7], s62 offen offset:0 lds\n"
      "s_add_u32 s62, s62, 2560\n"
      "s_waitcnt vmcnt(3)\n"
      "buffer_load_dwordx4 v2, s[4:7], s62 offen offset:1024 lds\n"
      "s_add_u32 s62, s62, 2560\n"
      "s_waitcnt vmcnt(3)\n"
      "buffer_load_dwordx4 v2, s[4:7], s62 offen offset:2048 lds\n"
      "s_add_u32 s62, s62, 2560\n"
      "s_waitcnt vmcnt(3)\n"
      "buffer_load_dwordx4 v2, s[4:7], s62 offen offset:3072 lds\n"
      "s_add_u32 s62, s62, 2560\n"
      "s_waitcnt vmcnt(3)\n"
      "s_add_u32 m0, m0, 0x1000\n"
      "s_add_u32 s62, s62, 0x1000\n"
      "buffer_load_dwordx4 v2, s[4:7], s62 offen offset:0 lds\n"
      "s_add_u32 s62, s62, 2560\n"
      "s_waitcnt vmcnt(3)\n"
      "buffer_load_dwordx4 v2, s[4:7], s62 offen offset:1024 lds\n"
      "s_add_u32 s62, s62, 2560\n"
      "s_waitcnt vmcnt(3)\n"
      "buffer_load_dwordx4 v2, s[4:7], s62 offen offset:2048 lds\n"
      "s_waitcnt vmcnt(3)\n"
      "s_waitcnt vmcnt(7)\n"
      "s_mov_b64 exec, 0xffff\n"
      "v_cmp_ne_u64 vcc, s[28:29], v[14:15]\n"
      "s_mov_b64 exec, -1\n"
      "s_cbranch_vccz .Lmk_post43\n"
      "s_mov_b32 s95, 131071\n"
      "s_mov_b64 s[86:87], 0xffff\n"
      "s_mov_b64 s[88:89], 0xffff\n"
      ".Lmk_poll44:\n"
      "s_mov_b64 exec, s[88:89]\n"
      "buffer_load_dwordx2 v[14:15], v241, s[12:15], 0 offen sc1\n"
      "s_waitcnt vmcnt(0)\n"
      "v_cmp_ne_u64 vcc, s[28:29], v[14:15]\n"
      "s_andn2_b64 s[86:87], s[86:87], s[88:89]\n"
      "s_or_b64 s[86:87], s[86:87], vcc\n"
      "s_cbranch_scc0 .Lmk_polld45\n"
      "s_ff1_i32_b64 s90, vcc\n"
      "s_lshl_b64 s[88:89], 1, s90\n"
      "s_cmp_eq_u64 vcc, 0\n"
      "s_cselect_b64 s[88:89], s[86:87], s[88:89]\n"
      "s_sleep 2\n"
      "s_sub_u32 s95, s95, 1\n"
      "s_cbranch_scc0 .Lmk_poll44\n"
      ".Lmk_polld45:\n"
      "s_mov_b64 exec, -1\n"
      ".Lmk_post43:\n"
      "ds_write_b32 v252, v252\n"
      "s_waitcnt vmcnt(7) lgkmcnt(0)\n"
      "s_mov_b64 exec, 1\n"
      "ds_add_u32 v10, v11\n"
      "s_mov_b64 exec, -1\n"
      "s_add_u32 s45, s45, 4\n"
      "s_mov_b32 s95, 1048576\n"
      ".Lmk_meet46:\n"
      "ds_read_b32 v12, v10\n"
      "s_waitcnt lgkmcnt(0)\n"
      "v_cmp_gt_u32 vcc, s45, v12\n"
      "s_cbranch_vccz .Lmk_meetd47\n"
      "s_sleep 2\n"
      "s_sub_u32 s95, s95, 1\n"
      "s_cbranch_scc0 .Lmk_meet46\n"
      ".Lmk_meetd47:\n"
      "ds_read_b64_tr_b16 v[48:49], v4 offset:28672\n"
      "ds_read_b64_tr_b16 v[50:51], v4 offset:30720\n"
      "ds_read_b64_tr_b16 v[52:53], v4 offset:32768\n"
      "ds_read_b64_tr_b16 v[54:55], v3 offset:34816\n"
      "ds_read_b64_tr_b16 v[56:57], v4 offset:35840\n"
      "ds_read_b64_tr_b16 v[58:59], v4 offset:37888\n"
      "ds_read_b64_tr_b16 v[60:61], v4 offset:39936\n"
      "ds_read_b64_tr_b16 v[62:63], v3 offset:41984\n"
      "ds_read_b64_tr_b16 v[64:65], v4 offset:43008\n"
      "ds_read_b64_tr_b16 v[66:67], v4 offset:45056\n"
      "ds_read_b64_tr_b16 v[68:69], v4 offset:47104\n"
      "ds_read_b64_tr_b16 v[70:71], v3 offset:49152\n"
      "ds_read_b64_tr_b16 v[72:73], v4 offset:50176\n"
      "ds_read_b64_tr_b16 v[74:75], v4 offset:52224\n"
      "ds_read_b64_tr_b16 v[76:77], v4 offset:54272\n"
      "ds_read_b64_tr_b16 v[78:79], v3 offset:56320\n"
      "buffer_store_dwordx4 v[248:251], v8, s[12:15], s60 offen sc1\n"
      "buffer_store_dwordx4 v[248:251], v8, s[12:15], s60 offen offset:1024 sc1\n"
      "s_add_u32 m0, s61, 0\n"
      "s_add_u32 s62, s43, 301056\n"
      "buffer_load_dwordx4 v2, s[4:7], s62 offen offset:0 lds\n"
      "s_add_u32 s62, s62, 2560\n"
      "s_waitcnt vmcnt(3)\n"
      "buffer_load_dwordx4 v2, s[4:7], s62 offen offset:1024 lds\n"
      "s_add_u32 s62, s62, 2560\n"
      "s_waitcnt vmcnt(3)\n"
      "buffer_load_dwordx4 v2, s[4:7], s62 offen offset:2048 lds\n"
      "s_add_u32 s62, s62, 2560\n"
      "s_waitcnt vmcnt(3)\n"
      "buffer_load_dwordx4 v2, s[4:7], s62 offen offset:3072 lds\n"
      "s_add_u32 s62, s62, 2560\n"
      "s_waitcnt vmcnt(3)\n"
      "s_add_u32 m0, m0, 0x1000\n"
      "s_add_u32 s62, s62, 0x1000\n"
      "buffer_load_dwordx4 v2, s[4:7], s62 offen offset:0 lds\n"
      "s_add_u32 s62, s62, 2560\n"
      "s_waitcnt vmcnt(3)\n"
      "buffer_load_dwordx4 v2, s[4:7], s62 offen offset:1024 lds\n"
      "s_add_u32 s62, s62, 2560\n"
      "s_waitcnt vmcnt(3)\n"
      "buffer_load_dwordx4 v2, s[4:7], s62 offen offset:2048 lds\n"
      "s_waitcnt vmcnt(3)\n"
      "s_waitcnt vmcnt(7)\n"
      "buffer_store_dwordx2 v[246:247], v244, s[12:15], 0 offen sc1\n"
      "s_waitcnt vmcnt(7) lgkmcnt(0)\n"
      "s_mov_b64 exec, 1\n"
      "ds_add_u32 v10, v11\n"
      "s_mov_b64 exec, -1\n"
      "s_add_u32 s45, s45, 4\n"
      "s_mov_b32 s95, 1048576\n"
      ".Lmk_meet48:\n"
      "ds_read_b32 v12, v10\n"
      "s_waitcnt lgkmcnt(0)\n"
      "v_cmp_gt_u32 vcc, s45, v12\n"
      "s_cbranch_vccz .Lmk_meetd49\n"
      "s_sleep 2\n"
      "s_sub_u32 s95, s95, 1\n"
      "s_cbranch_scc0 .Lmk_meet48\n"
      ".Lmk_meetd49:\n"
      "s_nop 0\n"
      "ds_read_b64_tr_b16 v[80:81], v253 offset:0\n"
      "ds_read_b64_tr_b16 v[82:83], v253 offset:2048\n"
      "ds_read_b64_tr_b16 v[84:85], v253 offset:4096\n"
      "ds_read_b64_tr_b16 v[86:87], v240 offset:6144\n"
      "ds_read_b64_tr_b16 v[88:89], v253 offset:7168\n"
      "ds_read_b64_tr_b16 v[90:91], v253 offset:9216\n"
      "ds_read_b64_tr_b16 v[92:93], v253 offset:11264\n"
      "ds_read_b64_tr_b16 v[94:95], v240 offset:13312\n"
      "ds_read_b64_tr_b16 v[96:97], v253 offset:14336\n"
      "ds_read_b64_tr_b16 v[98:99], v253 offset:16384\n"
      "ds_read_b64_tr_b16 v[100:101], v253 offset:18432\n"
      "ds_read_b64_tr_b16 v[102:103], v240 offset:20480\n"
      "ds_read_b64_tr_b16 v[104:105], v253 offset:21504\n"
      "ds_read_b64_tr_b16 v[106:107], v253 offset:23552\n"
      "ds_read_b64_tr_b16 v[108:109], v253 offset:25600\n"
      "ds_read_b64_tr_b16 v[110:111], v240 offset:27648\n"
      "s_add_u32 m0, s61, 28672\n"
      "s_add_u32 s62, s43, 0\n"
      "buffer_load_dwordx4 v2, s[8:11], s62 offen offset:0 lds\n"
      "s_add_u32 s62, s62, 2560\n"
      "s_waitcnt vmcnt(3)\n"
      "buffer_load_dwordx4 v2, s[8:11], s62 offen offset:1024 lds\n"
      "s_add_u32 s62, s62, 2560\n"
      "s_waitcnt vmcnt(3)\n"
      "buffer_load_dwordx4 v2, s[8:11], s62 offen offset:2048 lds\n"
      "s_add_u32 s62, s62, 2560\n"
      "s_waitcnt vmcnt(3)\n"
      "buffer_load_dwordx4 v2, s[8:11], s62 offen offset:3072 lds\n"
      "s_add_u32 s62, s62, 2560\n"
      "s_waitcnt vmcnt(3)\n"
      "s_add_u32 m0, m0, 0x1000\n"
      "s_add_u32 s62, s62, 0x1000\n"
      "buffer_load_dwordx4 v2, s[8:11], s62 offen offset:0 lds\n"
      "s_add_u32 s62, s62, 2560\n"
      "s_waitcnt vmcnt(3)\n"
      "buffer_load_dwordx4 v2, s[8:11], s62 offen offset:1024 lds\n"
      "s_add_u32 s62, s62, 2560\n"
      "s_waitcnt vmcnt(3)\n"
      "buffer_load_dwordx4 v2, s[8:11], s62 offen offset:2048 lds\n"
      "s_waitcnt vmcnt(3)\n"
      "s_waitcnt vmcnt(7) lgkmcnt(0)\n"
      "s_mov_b64 exec, 1\n"
      "ds_add_u32 v10, v11\n"
      "s_mov_b64 exec, -1\n"
      "s_add_u32 s45, s45, 4\n"
      "s_mov_b32 s95, 1048576\n"
      ".Lmk_meet50:\n"
      "ds_read_b32 v12, v10\n"
      "s_waitcnt lgkmcnt(0)\n"
      "v_cmp_gt_u32 vcc, s45, v12\n"
      "s_cbranch_vccz .Lmk_meetd51\n"
      "s_sleep 2\n"
      "s_sub_u32 s95, s95, 1\n"
      "s_cbranch_scc0 .Lmk_meet50\n"
      ".Lmk_meetd51:\n"
      "ds_read_b64_tr_b16 v[112:113], v4 offset:0\n"
      "ds_read_b64_tr_b16 v[114:115], v4 offset:2048\n"
      "ds_read_b64_tr_b16 v[116:117], v4 offset:4096\n"
      "ds_read_b64_tr_b16 v[118:119], v3 offset:6144\n"
      "ds_read_b64_tr_b16 v[120:121], v4 offset:7168\n"
      "ds_read_b64_tr_b16 v[122:123], v4 offset:9216\n"
      "ds_read_b64_tr_b16 v[124:125], v4 offset:11264\n"
      "ds_read_b64_tr_b16 v[126:127], v3 offset:13312\n"
      "ds_read_b64_tr_b16 v[128:129], v4 offset:14336\n"
      "ds_read_b64_tr_b16 v[130:131], v4 offset:16384\n"
      "ds_read_b64_tr_b16 v[132:133], v4 offset:18432\n"
      "ds_read_b64_tr_b16 v[134:135], v3 offset:20480\n"
      "ds_read_b64_tr_b16 v[136:137], v4 offset:21504\n"
      "ds_read_b64_tr_b16 v[138:139], v4 offset:23552\n"
      "ds_read_b64_tr_b16 v[140:141], v4 offset:25600\n"
      "ds_read_b64_tr_b16 v[142:143], v3 offset:27648\n"
      "s_add_u32 m0, s61, -32768\n"
      "s_add_u32 s62, s43, 100352\n"
      "buffer_load_dwordx4 v2, s[8:11], s62 offen offset:0 lds\n"
      "s_add_u32 s62, s62, 2560\n"
      "s_waitcnt vmcnt(3)\n"
      "buffer_load_dwordx4 v2, s[8:11], s62 offen offset:1024 lds\n"
      "s_add_u32 s62, s62, 2560\n"
      "s_waitcnt vmcnt(3)\n"
      "buffer_load_dwordx4 v2, s[8:11], s62 offen offset:2048 lds\n"
      "s_add_u32 s62, s62, 2560\n"
      "s_waitcnt vmcnt(3)\n"
      "buffer_load_dwordx4 v2, s[8:11], s62 offen offset:3072 lds\n"
      "s_add_u32 s62, s62, 2560\n"
      "s_waitcnt vmcnt(3)\n"
      "s_add_u32 m0, m0, 0x1000\n"
      "s_add_u32 s62, s62, 0x1000\n"
      "buffer_load_dwordx4 v2, s[8:11], s62 offen offset:0 lds\n"
      "s_add_u32 s62, s62, 2560\n"
      "s_waitcnt vmcnt(3)\n"
      "buffer_load_dwordx4 v2, s[8:11], s62 offen offset:1024 lds\n"
      "s_add_u32 s62, s62, 2560\n"
      "s_waitcnt vmcnt(3)\n"
      "buffer_load_dwordx4 v2, s[8:11], s62 offen offset:2048 lds\n"
      "s_waitcnt vmcnt(3)\n"
      "s_waitcnt vmcnt(7) lgkmcnt(0)\n"
      "s_mov_b64 exec, 1\n"
      "ds_add_u32 v10, v11\n"
      "s_mov_b64 exec, -1\n"
      "s_add_u32 s45, s45, 4\n"
      "s_mov_b32 s95, 1048576\n"
      ".Lmk_meet52:\n"
      "ds_read_b32 v12, v10\n"
      "s_waitcnt lgkmcnt(0)\n"
      "v_cmp_gt_u32 vcc, s45, v12\n"
      "s_cbranch_vccz .Lmk_meetd53\n"
      "s_sleep 2\n"
      "s_sub_u32 s95, s95, 1\n"
      "s_cbranch_scc0 .Lmk_meet52\n"
      ".Lmk_meetd53:\n"
      "s_nop 0\n"
      "ds_read_b64_tr_b16 v[144:145], v4 offset:28672\n"
      "ds_read_b64_tr_b16 v[146:147], v4 offset:30720\n"
      "ds_read_b64_tr_b16 v[148:149], v4 offset:32768\n"
      "ds_read_b64_tr_b16 v[150:151], v3 offset:34816\n"
      "ds_read_b64_tr_b16 v[152:153], v4 offset:35840\n"
      "ds_read_b64_tr_b16 v[154:155], v4 offset:37888\n"
      "ds_read_b64_tr_b16 v[156:157], v4 offset:39936\n"
      "ds_read_b64_tr_b16 v[158:159], v3 offset:41984\n"
      "ds_read_b64_tr_b16 v[160:161], v4 offset:43008\n"
      "ds_read_b64_tr_b16 v[162:163], v4 offset:45056\n"
      "ds_read_b64_tr_b16 v[164:165], v4 offset:47104\n"
      "ds_read_b64_tr_b16 v[166:167], v3 offset:49152\n"
      "ds_read_b64_tr_b16 v[168:169], v4 offset:50176\n"
      "ds_read_b64_tr_b16 v[170:171], v4 offset:52224\n"
      "ds_read_b64_tr_b16 v[172:173], v4 offset:54272\n"
      "ds_read_b64_tr_b16 v[174:175], v3 offset:56320\n"
      "s_add_u32 m0, s61, 0\n"
      "s_add_u32 s62, s43, 200704\n"
      "buffer_load_dwordx4 v2, s[8:11], s62 offen offset:0 lds\n"
      "s_add_u32 s62, s62, 2560\n"
      "s_waitcnt vmcnt(3)\n"
      "buffer_load_dwordx4 v2, s[8:11], s62 offen offset:1024 lds\n"
      "s_add_u32 s62, s62, 2560\n"
      "s_waitcnt vmcnt(3)\n"
      "buffer_load_dwordx4 v2, s[8:11], s62 offen offset:2048 lds\n"
      "s_add_u32 s62, s62, 2560\n"
      "s_waitcnt vmcnt(3)\n"
      "buffer_load_dwordx4 v2, s[8:11], s62 offen offset:3072 lds\n"
      "s_add_u32 s62, s62, 2560\n"
      "s_waitcnt vmcnt(3)\n"
      "s_add_u32 m0, m0, 0x1000\n"
      "s_add_u32 s62, s62, 0x1000\n"
      "buffer_load_dwordx4 v2, s[8:11], s62 offen offset:0 lds\n"
      "s_add_u32 s62, s62, 2560\n"
      "s_waitcnt vmcnt(3)\n"
      "buffer_load_dwordx4 v2, s[8:11], s62 offen offset:1024 lds\n"
      "s_add_u32 s62, s62, 2560\n"
      "s_waitcnt vmcnt(3)\n"
      "buffer_load_dwordx4 v2, s[8:11], s62 offen offset:2048 lds\n"
      "s_waitcnt vmcnt(3)\n"
      "s_waitcnt vmcnt(7) lgkmcnt(0)\n"
      "s_mov_b64 exec, 1\n"
      "ds_add_u32 v10, v11\n"
      "s_mov_b64 exec, -1\n"
      "s_add_u32 s45, s45, 4\n"
      "s_mov_b32 s95, 1048576\n"
      ".Lmk_meet54:\n"
      "ds_read_b32 v12, v10\n"
      "s_waitcnt lgkmcnt(0)\n"
      "v_cmp_gt_u32 vcc, s45, v12\n"
      "s_cbranch_vccz .Lmk_meetd55\n"
      "s_sleep 2\n"
      "s_sub_u32 s95, s95, 1\n"
      "s_cbranch_scc0 .Lmk_meet54\n"
      ".Lmk_meetd55:\n"
      "ds_read_b64_tr_b16 v[176:177], v253 offset:0\n"
      "ds_read_b64_tr_b16 v[178:179], v253 offset:2048\n"
      "ds_read_b64_tr_b16 v[180:181], v253 offset:4096\n"
      "ds_read_b64_tr_b16 v[182:183], v240 offset:6144\n"
      "ds_read_b64_tr_b16 v[184:185], v253 offset:7168\n"
      "ds_read_b64_tr_b16 v[186:187], v253 offset:9216\n"
      "ds_read_b64_tr_b16 v[188:189], v253 offset:11264\n"
      "ds_read_b64_tr_b16 v[190:191], v240 offset:13312\n"
      "ds_read_b64_tr_b16 v[192:193], v253 offset:14336\n"
      "ds_read_b64_tr_b16 v[194:195], v253 offset:16384\n"
      "ds_read_b64_tr_b16 v[196:197], v253 offset:18432\n"
      "ds_read_b64_tr_b16 v[198:199], v240 offset:20480\n"
      "ds_read_b64_tr_b16 v[200:201], v253 offset:21504\n"
      "ds_read_b64_tr_b16 v[202:203], v253 offset:23552\n"
      "ds_read_b64_tr_b16 v[204:205], v253 offset:25600\n"
      "ds_read_b64_tr_b16 v[206:207], v240 offset:27648\n"
      "s_mov_b64 exec, 15\n"
      "buffer_load_dwordx2 v[14:15], v242, s[12:15], 0 offen sc1\n"
      "s_mov_b64 exec, -1\n"
      "s_add_u32 m0, s61, 28672\n"
      "s_add_u32 s62, s43, 301056\n"
      "buffer_load_dwordx4 v2, s[8:11], s62 offen offset:0 lds\n"
      "s_add_u32 s62, s62, 2560\n"
      "s_waitcnt vmcnt(3)\n"
      "buffer_load_dwordx4 v2, s[8:11], s62 offen offset:1024 lds\n"
      "s_add_u32 s62, s62, 2560\n"
      "s_waitcnt vmcnt(3)\n"
      "buffer_load_dwordx4 v2, s[8:11], s62 offen offset:2048 lds\n"
      "s_add_u32 s62, s62, 2560\n"
      "s_waitcnt vmcnt(3)\n"
      "buffer_load_dwordx4 v2, s[8:11], s62 offen offset:3072 lds\n"
      "s_add_u32 s62, s62, 2560\n"
      "s_waitcnt vmcnt(3)\n"
      "s_add_u32 m0, m0, 0x1000\n"
      "s_add_u32 s62, s62, 0x1000\n"
      "buffer_load_dwordx4 v2, s[8:11], s62 offen offset:0 lds\n"
      "s_add_u32 s62, s62, 2560\n"
      "s_waitcnt vmcnt(3)\n"
      "buffer_load_dwordx4 v2, s[8:11], s62 offen offset:1024 lds\n"
      "s_add_u32 s62, s62, 2560\n"
      "s_waitcnt vmcnt(3)\n"
      "buffer_load_dwordx4 v2, s[8:11], s62 offen offset:2048 lds\n"
      "s_waitcnt vmcnt(3)\n"
      "s_waitcnt vmcnt(7)\n"
      "s_mov_b64 exec, 15\n"
      "v_cmp_ne_u64 vcc, s[28:29], v[14:15]\n"
      "s_mov_b64 exec, -1\n"
      "s_cbranch_vccz .Lmk_post56\n"
      "s_mov_b32 s95, 131071\n"
      "s_mov_b64 s[86:87], 15\n"
      "s_mov_b64 s[88:89], 15\n"
      ".Lmk_poll57:\n"
      "s_mov_b64 exec, s[88:89]\n"
      "buffer_load_dwordx2 v[14:15], v242, s[12:15], 0 offen sc1\n"
      "s_waitcnt vmcnt(0)\n"
      "v_cmp_ne_u64 vcc, s[28:29], v[14:15]\n"
      "s_andn2_b64 s[86:87], s[86:87], s[88:89]\n"
      "s_or_b64 s[86:87], s[86:87], vcc\n"
      "s_cbranch_scc0 .Lmk_polld58\n"
      "s_ff1_i32_b64 s90, vcc\n"
      "s_lshl_b64 s[88:89], 1, s90\n"
      "s_cmp_eq_u64 vcc, 0\n"
      "s_cselect_b64 s[88:89], s[86:87], s[88:89]\n"
      "s_sleep 2\n"
      "s_sub_u32 s95, s95, 1\n"
      "s_cbranch_scc0 .Lmk_poll57\n"
      ".Lmk_polld58:\n"
      "s_mov_b64 exec, -1\n"
      ".Lmk_post56:\n"
      "s_cmp_lg_u32 s2, 0\n"
      "s_cbranch_scc1 .Lmk_noep\n"
      "s_and_b32 s55, s38, 1\n"
      "s_lshl_b32 s55, s55, 2\n"
      "v_lshrrev_b32 v13, 4, v1\n"
      "v_add_u32 v13, s55, v13\n"
      "v_and_b32 v12, 15, v1\n"
      "s_cmp_lt_u32 s38, 2\n"
      "s_cselect_b32 s55, 96, 1\n"
      "s_cselect_b32 s56, 1, 16\n"
      "s_cselect_b32 s63, 0, 196608\n"
      "v_mul_u32_u24 v13, s55, v13\n"
      "v_mad_u32_u24 v12, v12, s56, v13\n"
      "s_add_u32 s63, s63, 200704\n"
      "v_lshl_add_u32 v12, v12, 8, s63\n"
      "s_mov_b32 s95, 131071\n"
      "s_mov_b64 s[86:87], -1\n"
      "s_mov_b64 s[88:89], -1\n"
      ".Lmk_poll59:\n"
      "s_mov_b64 exec, s[88:89]\n"
      "buffer_load_dwordx2 v[14:15], v12, s[12:15], 0 offen sc1\n"
      "s_waitcnt vmcnt(0)\n"
      "v_cmp_ne_u64 vcc, s[28:29], v[14:15]\n"
      "s_andn2_b64 s[86:87], s[86:87], s[88:89]\n"
      "s_or_b64 s[86:87], s[86:87], vcc\n"
      "s_cbranch_scc0 .Lmk_polld60\n"
      "s_ff1_i32_b64 s90, vcc\n"
      "s_lshl_b64 s[88:89], 1, s90\n"
      "s_cmp_eq_u64 vcc, 0\n"
      "s_cselect_b64 s[88:89], s[86:87], s[88:89]\n"
      "s_sleep 2\n"
      "s_sub_u32 s95, s95, 1\n"
      "s_cbranch_scc0 .Lmk_poll59\n"
      ".Lmk_polld60:\n"
      "s_mov_b64 exec, -1\n"
      ".Lmk_noep:\n"
      "s_waitcnt vmcnt(7) lgkmcnt(0)\n"
      "s_mov_b64 exec, 1\n"
      "ds_add_u32 v10, v11\n"
      "s_mov_b64 exec, -1\n"
      "s_add_u32 s45, s45, 4\n"
      "s_mov_b32 s95, 1048576\n"
      ".Lmk_meet61:\n"
      "ds_read_b32 v12, v10\n"
      "s_waitcnt lgkmcnt(0)\n"
      "v_cmp_gt_u32 vcc, s45, v12\n"
      "s_cbranch_vccz .Lmk_meetd62\n"
      "s_sleep 2\n"
      "s_sub_u32 s95, s95, 1\n"
      "s_cbranch_scc0 .Lmk_meet61\n"
      ".Lmk_meetd62:\n"
      "s_or_b32 s55, s2, s38\n"
      "s_cbranch_scc1 .Lmk_noep2\n"
      "v_mov_b32_e64 v12, 0\n"
      "s_mov_b64 exec, 1\n"
      "buffer_store_dwordx2 v[246:247], v12, s[12:15], 0 offen sc1\n"
      "s_mov_b64 exec, -1\n"
      ".Lmk_noep2:\n"
      "s_mov_b64 exec, 15\n"
      "buffer_load_dwordx2 v[14:15], v243, s[12:15], 0 offen sc1\n"
      "s_mov_b64 exec, -1\n"
      "s_lshl_b32 s55, s38, 13\n"
      "s_add_u32 m0, s55, 49152\n"
      "s_nop 0\n"
      "buffer_load_dwordx4 v8, s[12:15], s59 offen offset:0 sc1 lds\n"
      "s_waitcnt vmcnt(3)\n"
      "buffer_load_dwordx4 v8, s[12:15], s59 offen offset:1024 sc1 lds\n"
      "s_waitcnt vmcnt(3)\n"
      "buffer_load_dwordx4 v8, s[12:15], s59 offen offset:2048 sc1 lds\n"
      "s_waitcnt vmcnt(3)\n"
      "buffer_load_dwordx4 v8, s[12:15], s59 offen offset:3072 sc1 lds\n"
      "s_waitcnt vmcnt(3)\n"
      "s_add_u32 m0, m0, 0x1000\n"
      "s_add_u32 s59, s59, 0x1000\n"
      "buffer_load_dwordx4 v8, s[12:15], s59 offen offset:0 sc1 lds\n"
      "s_waitcnt vmcnt(3)\n"
      "buffer_load_dwordx4 v8, s[12:15], s59 offen offset:1024 sc1 lds\n"
      "s_waitcnt vmcnt(3)\n"
      "buffer_load_dwordx4 v8, s[12:15], s59 offen offset:2048 sc1 lds\n"
      "s_waitcnt vmcnt(3)\n"
      "buffer_load_dwordx4 v8, s[12:15], s59 offen offset:3072 sc1 lds\n"
      "s_waitcnt vmcnt(3)\n"
      "s_waitcnt vmcnt(7)\n"
      "s_mov_b64 exec, 15\n"
      "v_cmp_ne_u64 vcc, s[28:29], v[14:15]\n"
      "s_mov_b64 exec, -1\n"
      "s_cbranch_vccz .Lmk_post63\n"
      "s_mov_b32 s95, 131071\n"
      "s_mov_b64 s[86:87], 15\n"
      "s_mov_b64 s[88:89], 15\n"
      ".Lmk_poll64:\n"
      "s_mov_b64 exec, s[88:89]\n"
      "buffer_load_dwordx2 v[14:15], v243, s[12:15], 0 offen sc1\n"
      "s_waitcnt vmcnt(0)\n"
      "v_cmp_ne_u64 vcc, s[28:29], v[14:15]\n"
      "s_andn2_b64 s[86:87], s[86:87], s[88:89]\n"
      "s_or_b64 s[86:87], s[86:87], vcc\n"
      "s_cbranch_scc0 .Lmk_polld65\n"
      "s_ff1_i32_b64 s90, vcc\n"
      "s_lshl_b64 s[88:89], 1, s90\n"
      "s_cmp_eq_u64 vcc, 0\n"
      "s_cselect_b64 s[88:89], s[86:87], s[88:89]\n"
      "s_sleep 2\n"
      "s_sub_u32 s95, s95, 1\n"
      "s_cbranch_scc0 .Lmk_poll64\n"
      ".Lmk_polld65:\n"
      "s_mov_b64 exec, -1\n"
      ".Lmk_post63:\n"
      "s_waitcnt vmcnt(0) lgkmcnt(0)\n"
      "s_barrier\n"
      "s_setprio 0\n"
      "s_barrier\n"
      "ds_read_b128 v[224:227], v5 offset:16384\n"
      "ds_read_b128 v[228:231], v5 offset:17408\n"
      "ds_read_b128 v[232:235], v5 offset:18432\n"
      "ds_read_b128 v[236:239], v5 offset:19456\n"
      "s_waitcnt lgkmcnt(3)\n"
      "v_mfma_f32_32x32x16_bf16 v[208:223], v[80:83], v[224:227], 0\n"
      "ds_read_b128 v[224:227], v5 offset:20480\n"
      "s_waitcnt lgkmcnt(3)\n"
      "v_mfma_f32_32x32x16_bf16 v[208:223], v[84:87], v[228:231], v[208:223]\n"
      "ds_read_b128 v[228:231], v5 offset:21504\n"
      "s_waitcnt lgkmcnt(3)\n"
      "v_mfma_f32_32x32x16_bf16 v[208:223], v[88:91], v[232:235], v[208:223]\n"
      "ds_read_b128 v[232:235], v5 offset:22528\n"
      "s_waitcnt lgkmcnt(3)\n"
      "v_mfma_f32_32x32x16_bf16 v[208:223], v[92:95], v[236:239], v[208:223]\n"
      "ds_read_b128 v[236:239], v5 offset:23552\n"
      "s_waitcnt lgkmcnt(3)\n"
      "v_mfma_f32_32x32x16_bf16 v[208:223], v[96:99], v[224:227], v[208:223]\n"
      "ds_read_b128 v[224:227], v5 offset:24576\n"
      "s_waitcnt lgkmcnt(3)\n"
      "v_mfma_f32_32x32x16_bf16 v[208:223], v[100:103], v[228:231], v[208:223]\n"
      "ds_read_b128 v[228:231], v5 offset:25600\n"
      "s_waitcnt lgkmcnt(3)\n"
      "v_mfma_f32_32x32x16_bf16 v[208:223], v[104:107], v[232:235], v[208:223]\n"
      "ds_read_b128 v[232:235], v5 offset:26624\n"
      "s_waitcnt lgkmcnt(3)\n"
      "v_mfma_f32_32x32x16_bf16 v[208:223], v[108:111], v[236:239], v[208:223]\n"
      "ds_read_b128 v[236:239], v5 offset:27648\n"
      "s_waitcnt lgkmcnt(3)\n"
      "v_mfma_f32_32x32x16_bf16 v[208:223], v[112:115], v[224:227], v[208:223]\n"
      "ds_read_b128 v[224:227], v5 offset:28672\n"
      "s_waitcnt lgkmcnt(3)\n"
      "v_mfma_f32_32x32x16_bf16 v[208:223], v[116:119], v[228:231], v[208:223]\n"
      "ds_read_b128 v[228:231], v5 offset:29696\n"
      "s_waitcnt lgkmcnt(3)\n"
      "v_mfma_f32_32x32x16_bf16 v[208:223], v[120:123], v[232:235], v[208:223]\n"
      "ds_read_b128 v[232:235], v5 offset:30720\n"
      "s_waitcnt lgkmcnt(3)\n"
      "v_mfma_f32_32x32x16_bf16 v[208:223], v[124:127], v[236:239], v[208:223]\n"
      "ds_read_b128 v[236:239], v5 offset:31744\n"
      "s_waitcnt lgkmcnt(3)\n"
      "v_mfma_f32_32x32x16_bf16 v[208:223], v[128:131], v[224:227], v[208:223]\n"
      "ds_read_b128 v[224:227], v5 offset:0\n"
      "s_waitcnt lgkmcnt(3)\n"
      "v_mfma_f32_32x32x16_bf16 v[208:223], v[132:135], v[228:231], v[208:223]\n"
      "ds_read_b128 v[228:231], v5 offset:1024\n"
      "s_waitcnt lgkmcnt(3)\n"
      "v_mfma_f32_32x32x16_bf16 v[208:223], v[136:139], v[232:235], v[208:223]\n"
      "ds_read_b128 v[232:235], v5 offset:2048\n"
      "s_waitcnt lgkmcnt(3)\n"
      "v_mfma_f32_32x32x16_bf16 v[208:223], v[140:143], v[236:239], v[208:223]\n"
      "ds_read_b128 v[236:239], v5 offset:3072\n"
      "s_waitcnt lgkmcnt(3)\n"
      "v_mfma_f32_32x32x16_bf16 v[208:223], v[16:19], v[224:227], v[208:223]\n"
      "ds_read_b64_tr_b16 v[80:81], v4 offset:0\n"
      "ds_read_b64_tr_b16 v[82:83], v4 offset:2048\n"
      "ds_read_b128 v[224:227], v5 offset:4096\n"
      "s_waitcnt lgkmcnt(5)\n"
      "v_mfma_f32_32x32x16_bf16 v[208:223], v[20:23], v[228:231], v[208:223]\n"
      "ds_read_b64_tr_b16 v[84:85], v4 offset:4096\n"
      "ds_read_b64_tr_b16 v[86:87], v3 offset:6144\n"
      "ds_read_b128 v[228:231], v5 offset:5120\n"
      "s_waitcnt lgkmcnt(7)\n"
      "v_mfma_f32_32x32x16_bf16 v[208:223], v[24:27], v[232:235], v[208:223]\n"
      "ds_read_b64_tr_b16 v[88:89], v4 offset:7168\n"
      "ds_read_b64_tr_b16 v[90:91], v4 offset:9216\n"
      "ds_read_b128 v[232:235], v5 offset:6144\n"
      "s_waitcnt lgkmcnt(9)\n"
      "v_mfma_f32_32x32x16_bf16 v[208:223], v[28:31], v[236:239], v[208:223]\n"
      "ds_read_b64_tr_b16 v[92:93], v4 offset:11264\n"
      "ds_read_b64_tr_b16 v[94:95], v3 offset:13312\n"
      "ds_read_b128 v[236:239], v5 offset:7168\n"
      "s_waitcnt lgkmcnt(9)\n"
      "v_mfma_f32_32x32x16_bf16 v[208:223], v[32:35], v[224:227], v[208:223]\n"
      "ds_read_b64_tr_b16 v[96:97], v4 offset:14336\n"
      "ds_read_b64_tr_b16 v[98:99], v4 offset:16384\n"
      "ds_read_b128 v[224:227], v5 offset:8192\n"
      "s_waitcnt lgkmcnt(9)\n"
      "v_mfma_f32_32x32x16_bf16 v[208:223], v[36:39], v[228:231], v[208:223]\n"
      "ds_read_b64_tr_b16 v[100:101], v4 offset:18432\n"
      "ds_read_b64_tr_b16 v[102:103], v3 offset:20480\n"
      "ds_read_b128 v[228:231], v5 offset:9216\n"
      "s_waitcnt lgkmcnt(9)\n"
      "v_mfma_f32_32x32x16_bf16 v[208:223], v[40:43], v[232:235], v[208:223]\n"
      "ds_read_b64_tr_b16 v[104:105], v4 offset:21504\n"
      "ds_read_b64_tr_b16 v[106:107], v4 offset:23552\n"
      "ds_read_b128 v[232:235], v5 offset:10240\n"
      "s_waitcnt lgkmcnt(9)\n"
      "v_mfma_f32_32x32x16_bf16 v[208:223], v[44:47], v[236:239], v[208:223]\n"
      "ds_read_b64_tr_b16 v[108:109], v4 offset:25600\n"
      "ds_read_b64_tr_b16 v[110:111], v3 offset:27648\n"
      "ds_read_b128 v[236:239], v5 offset:11264\n"
      "s_waitcnt lgkmcnt(9)\n"
      "v_mfma_f32_32x32x16_bf16 v[208:223], v[48:51], v[224:227], v[208:223]\n"
      "ds_read_b64_tr_b16 v[112:113], v4 offset:28672\n"
      "ds_read_b64_tr_b16 v[114:115], v4 offset:30720\n"
      "ds_read_b128 v[224:227], v5 offset:12288\n"
      "s_waitcnt lgkmcnt(9)\n"
      "v_mfma_f32_32x32x16_bf16 v[208:223], v[52:55], v[228:231], v[208:223]\n"
      "ds_read_b64_tr_b16 v[116:117], v4 offset:32768\n"
      "ds_read_b64_tr_b16 v[118:119], v3 offset:34816\n"
      "ds_read_b128 v[228:231], v5 offset:13312\n"
      "s_waitcnt lgkmcnt(9)\n"
      "v_mfma_f32_32x32x16_bf16 v[208:223], v[56:59], v[232:235], v[208:223]\n"
      "ds_read_b64_tr_b16 v[120:121], v4 offset:35840\n"
      "ds_read_b64_tr_b16 v[122:123], v4 offset:37888\n"
      "ds_read_b128 v[232:235], v5 offset:14336\n"
      "s_waitcnt lgkmcnt(9)\n"
      "v_mfma_f32_32x32x16_bf16 v[208:223], v[60:63], v[236:239], v[208:223]\n"
      "ds_read_b64_tr_b16 v[124:125], v4 offset:39936\n"
      "ds_read_b64_tr_b16 v[126:127], v3 offset:41984\n"
      "ds_read_b128 v[236:239], v5 offset:15360\n"
      "s_waitcnt lgkmcnt(9)\n"
      "v_mfma_f32_32x32x16_bf16 v[208:223], v[64:67], v[224:227], v[208:223]\n"
      "ds_read_b64_tr_b16 v[128:129], v4 offset:43008\n"
      "ds_read_b64_tr_b16 v[130:131], v4 offset:45056\n"
      "s_waitcnt lgkmcnt(8)\n"
      "v_mfma_f32_32x32x16_bf16 v[208:223], v[68:71], v[228:231], v[208:223]\n"
      "ds_read_b64_tr_b16 v[132:133], v4 offset:47104\n"
      ".Lmk_pad5:\n"
      "s_cbranch_execz .Lmk_padr5\n"
      "s_nop 0\n"
      "ds_read_b64_tr_b16 v[134:135], v3 offset:49152\n"
      "s_waitcnt lgkmcnt(7)\n"
      "v_mfma_f32_32x32x16_bf16 v[208:223], v[72:75], v[232:235], v[208:223]\n"
      "ds_read_b64_tr_b16 v[136:137], v4 offset:50176\n"
      "ds_read_b64_tr_b16 v[138:139], v4 offset:52224\n"
      "s_waitcnt lgkmcnt(6)\n"
      "v_mfma_f32_32x32x16_bf16 v[208:223], v[76:79], v[236:239], v[208:223]\n"
      "ds_read_b64_tr_b16 v[140:141], v4 offset:54272\n"
      "ds_read_b64_tr_b16 v[142:143], v3 offset:56320\n"
      "v_and_b32 v12, 3, v1\n"
      "v_lshlrev_b32_e64 v12, 2, v12\n"
      "v_add_u32 v12, 161840, v12\n"
      "s_mov_b32 s63, 15\n"
      "s_mov_b32 s95, 1048576\n"
      "s_nop 4\n"
      "v_cvt_pk_bf16_f32 v224, v208, v209\n"
      "v_cvt_pk_bf16_f32 v225, v210, v211\n"
      "v_cvt_pk_bf16_f32 v226, v212, v213\n"
      "v_cvt_pk_bf16_f32 v227, v214, v215\n"
      "v_cvt_pk_bf16_f32 v228, v216, v217\n"
      "v_cvt_pk_bf16_f32 v229, v218, v219\n"
      "v_cvt_pk_bf16_f32 v230, v220, v221\n"
      "v_cvt_pk_bf16_f32 v231, v222, v223\n"
      "ds_write_b64 v6, v[224:225] offset:0\n"
      "ds_write_b64 v6, v[226:227] offset:16\n"
      "ds_write_b64 v6, v[228:229] offset:32\n"
      "ds_write_b64 v6, v[230:231] offset:48\n"
      "s_mov_b64 exec, 1\n"
      "ds_add_u32 v10, v11 offset:4\n"
      "s_mov_b64 exec, -1\n"
      ".Lmk_disp:\n"
      "ds_read_b32 v13, v12\n"
      "s_waitcnt lgkmcnt(0)\n"
      "v_cmp_ne_u32 vcc, 0, v13\n"
      "s_nop 0\n"
      "s_and_b32 s62, vcc_lo, s63\n"
      "s_cbranch_scc1 .Lmk_go\n"
      "s_sub_u32 s95, s95, 1\n"
      "s_cbranch_scc0 .Lmk_disp\n"
      "s_mov_b32 s62, s63\n"
      ".Lmk_go:\n"
      "s_andn2_b32 s63, s63, s62\n"
      "s_bitcmp0_b32 s62, 0\n"
      "s_cbranch_scc1 .Lmk_nq66\n"
      "s_nop 0\n"
      "ds_read_b128 v[208:211], v8 offset:0\n"
      "ds_read_b128 v[212:215], v8 offset:1024\n"
      "ds_read_b128 v[216:219], v8 offset:2048\n"
      "ds_read_b128 v[220:223], v8 offset:3072\n"
      "ds_read_b128 v[224:227], v8 offset:4096\n"
      "ds_read_b128 v[228:231], v8 offset:5120\n"
      "ds_read_b128 v[232:235], v8 offset:6144\n"
      "ds_read_b128 v[236:239], v8 offset:7168\n"
      "s_waitcnt lgkmcnt(7)\n"
      "v_mfma_f32_32x32x16_bf16 v[16:31], v[144:147], v[208:211], 0\n"
      "s_waitcnt lgkmcnt(6)\n"
      "v_mfma_f32_32x32x16_bf16 v[16:31], v[148:151], v[212:215], v[16:31]\n"
      "s_waitcnt lgkmcnt(5)\n"
      "v_mfma_f32_32x32x16_bf16 v[16:31], v[152:155], v[216:219], v[16:31]\n"
      "s_waitcnt lgkmcnt(4)\n"
      "v_mfma_f32_32x32x16_bf16 v[16:31], v[156:159], v[220:223], v[16:31]\n"
      "s_waitcnt lgkmcnt(3)\n"
      "v_mfma_f32_32x32x16_bf16 v[16:31], v[160:163], v[224:227], v[16:31]\n"
      "s_waitcnt lgkmcnt(2)\n"
      "v_mfma_f32_32x32x16_bf16 v[16:31], v[164:167], v[228:231], v[16:31]\n"
      "s_waitcnt lgkmcnt(1)\n"
      "v_mfma_f32_32x32x16_bf16 v[16:31], v[168:171], v[232:235], v[16:31]\n"
      "s_waitcnt lgkmcnt(0)\n"
      "v_mfma_f32_32x32x16_bf16 v[16:31], v[172:175], v[236:239], v[16:31]\n"
      ".Lmk_nq66:\n"
      "s_bitcmp0_b32 s62, 1\n"
      "s_cbranch_scc1 .Lmk_nq67\n"
      "ds_read_b128 v[208:211], v8 offset:12288\n"
      "ds_read_b128 v[212:215], v8 offset:13312\n"
      "ds_read_b128 v[216:219], v8 offset:14336\n"
      "ds_read_b128 v[220:223], v8 offset:15360\n"
      "ds_read_b128 v[224:227], v8 offset:16384\n"
      "ds_read_b128 v[228:231], v8 offset:17408\n"
      "ds_read_b128 v[232:235], v8 offset:18432\n"
      "ds_read_b128 v[236:239], v8 offset:19456\n"
      "s_waitcnt lgkmcnt(7)\n"
      "v_mfma_f32_32x32x16_bf16 v[32:47], v[176:179], v[208:211], 0\n"
      "s_waitcnt lgkmcnt(6)\n"
      "v_mfma_f32_32x32x16_bf16 v[32:47], v[180:183], v[212:215], v[32:47]\n"
      "s_waitcnt lgkmcnt(5)\n"
      "v_mfma_f32_32x32x16_bf16 v[32:47], v[184:187], v[216:219], v[32:47]\n"
      "s_waitcnt lgkmcnt(4)\n"
      "v_mfma_f32_32x32x16_bf16 v[32:47], v[188:191], v[220:223], v[32:47]\n"
      "s_waitcnt lgkmcnt(3)\n"
      "v_mfma_f32_32x32x16_bf16 v[32:47], v[192:195], v[224:227], v[32:47]\n"
      "s_waitcnt lgkmcnt(2)\n"
      "v_mfma_f32_32x32x16_bf16 v[32:47], v[196:199], v[228:231], v[32:47]\n"
      "s_waitcnt lgkmcnt(1)\n"
      "v_mfma_f32_32x32x16_bf16 v[32:47], v[200:203], v[232:235], v[32:47]\n"
      "s_waitcnt lgkmcnt(0)\n"
      "v_mfma_f32_32x32x16_bf16 v[32:47], v[204:207], v[236:239], v[32:47]\n"
      ".Lmk_nq67:\n"
      "s_bitcmp0_b32 s62, 2\n"
      "s_cbranch_scc1 .Lmk_nq68\n"
      "ds_read_b128 v[208:211], v8 offset:24576\n"
      "ds_read_b128 v[212:215], v8 offset:25600\n"
      "ds_read_b128 v[216:219], v8 offset:26624\n"
      "ds_read_b128 v[220:223], v8 offset:27648\n"
      "ds_read_b128 v[224:227], v8 offset:28672\n"
      "ds_read_b128 v[228:231], v8 offset:29696\n"
      "ds_read_b128 v[232:235], v8 offset:30720\n"
      "ds_read_b128 v[236:239], v8 offset:31744\n"
      "s_waitcnt lgkmcnt(7)\n"
      "v_mfma_f32_32x32x16_bf16 v[48:63], v[80:83], v[208:211], 0\n"
      "s_waitcnt lgkmcnt(6)\n"
      "v_mfma_f32_32x32x16_bf16 v[48:63], v[84:87], v[212:215], v[48:63]\n"
      "s_waitcnt lgkmcnt(5)\n"
      "v_mfma_f32_32x32x16_bf16 v[48:63], v[88:91], v[216:219], v[48:63]\n"
      "s_waitcnt lgkmcnt(4)\n"
      "v_mfma_f32_32x32x16_bf16 v[48:63], v[92:95], v[220:223], v[48:63]\n"
      "s_waitcnt lgkmcnt(3)\n"
      "v_mfma_f32_32x32x16_bf16 v[48:63], v[96:99], v[224:227], v[48:63]\n"
      "s_waitcnt lgkmcnt(2)\n"
      "v_mfma_f32_32x32x16_bf16 v[48:63], v[100:103], v[228:231], v[48:63]\n"
      "s_waitcnt lgkmcnt(1)\n"
      "v_mfma_f32_32x32x16_bf16 v[48:63], v[104:107], v[232:235], v[48:63]\n"
      "s_waitcnt lgkmcnt(0)\n"
      "v_mfma_f32_32x32x16_bf16 v[48:63], v[108:111], v[236:239], v[48:63]\n"
      ".Lmk_nq68:\n"
      "s_bitcmp0_b32 s62, 3\n"
      "s_cbranch_scc1 .Lmk_nq69\n"
      "ds_read_b128 v[208:211], v8 offset:36864\n"
      "ds_read_b128 v[212:215], v8 offset:37888\n"
      "ds_read_b128 v[216:219], v8 offset:38912\n"
      "ds_read_b128 v[220:223], v8 offset:39936\n"
      "ds_read_b128 v[224:227], v8 offset:40960\n"
      "ds_read_b128 v[228:231], v8 offset:41984\n"
      "ds_read_b128 v[232:235], v8 offset:43008\n"
      "ds_read_b128 v[236:239], v8 offset:44032\n"
      "s_waitcnt lgkmcnt(7)\n"
      "v_mfma_f32_32x32x16_bf16 v[64:79], v[112:115], v[208:211], 0\n"
      "s_waitcnt lgkmcnt(6)\n"
      "v_mfma_f32_32x32x16_bf16 v[64:79], v[116:119], v[212:215], v[64:79]\n"
      "s_waitcnt lgkmcnt(5)\n"
      "v_mfma_f32_32x32x16_bf16 v[64:79], v[120:123], v[216:219], v[64:79]\n"
      "s_waitcnt lgkmcnt(4)\n"
      "v_mfma_f32_32x32x16_bf16 v[64:79], v[124:127], v[220:223], v[64:79]\n"
      "s_waitcnt lgkmcnt(3)\n"
      "v_mfma_f32_32x32x16_bf16 v[64:79], v[128:131], v[224:227], v[64:79]\n"
      "s_waitcnt lgkmcnt(2)\n"
      "v_mfma_f32_32x32x16_bf16 v[64:79], v[132:135], v[228:231], v[64:79]\n"
      "s_waitcnt lgkmcnt(1)\n"
      "v_mfma_f32_32x32x16_bf16 v[64:79], v[136:139], v[232:235], v[64:79]\n"
      "s_waitcnt lgkmcnt(0)\n"
      "v_mfma_f32_32x32x16_bf16 v[64:79], v[140:143], v[236:239], v[64:79]\n"
      ".Lmk_nq69:\n"
      "s_cmp_lg_u32 s63, 0\n"
      "s_cbranch_scc1 .Lmk_disp\n"
      "s_waitcnt lgkmcnt(0)\n"
      "s_nop 4\n"
      "v_pk_add_f32 v[16:17], v[16:17], v[32:33]\n"
      "v_pk_add_f32 v[18:19], v[18:19], v[34:35]\n"
      "v_pk_add_f32 v[20:21], v[20:21], v[36:37]\n"
      "v_pk_add_f32 v[22:23], v[22:23], v[38:39]\n"
      "v_pk_add_f32 v[24:25], v[24:25], v[40:41]\n"
      "v_pk_add_f32 v[26:27], v[26:27], v[42:43]\n"
      "v_pk_add_f32 v[28:29], v[28:29], v[44:45]\n"
      "v_pk_add_f32 v[30:31], v[30:31], v[46:47]\n"
      "v_pk_add_f32 v[48:49], v[48:49], v[64:65]\n"
      "v_pk_add_f32 v[50:51], v[50:51], v[66:67]\n"
      "v_pk_add_f32 v[52:53], v[52:53], v[68:69]\n"
      "v_pk_add_f32 v[54:55], v[54:55], v[70:71]\n"
      "v_pk_add_f32 v[56:57], v[56:57], v[72:73]\n"
      "v_pk_add_f32 v[58:59], v[58:59], v[74:75]\n"
      "v_pk_add_f32 v[60:61], v[60:61], v[76:77]\n"
      "v_pk_add_f32 v[62:63], v[62:63], v[78:79]\n"
      "v_pk_add_f32 v[16:17], v[16:17], v[48:49]\n"
      "v_pk_add_f32 v[18:19], v[18:19], v[50:51]\n"
      "v_pk_add_f32 v[20:21], v[20:21], v[52:53]\n"
      "v_pk_add_f32 v[22:23], v[22:23], v[54:55]\n"
      "v_pk_add_f32 v[24:25], v[24:25], v[56:57]\n"
      "v_pk_add_f32 v[26:27], v[26:27], v[58:59]\n"
      "v_pk_add_f32 v[28:29], v[28:29], v[60:61]\n"
      "v_pk_add_f32 v[30:31], v[30:31], v[62:63]\n"
      "v_cvt_pk_bf16_f32 v224, v16, v17\n"
      "v_cvt_pk_bf16_f32 v225, v18, v19\n"
      "v_cvt_pk_bf16_f32 v226, v20, v21\n"
      "v_cvt_pk_bf16_f32 v227, v22, v23\n"
      "v_cvt_pk_bf16_f32 v228, v24, v25\n"
      "v_cvt_pk_bf16_f32 v229, v26, v27\n"
      "v_cvt_pk_bf16_f32 v230, v28, v29\n"
      "v_cvt_pk_bf16_f32 v231, v30, v31\n"
      "ds_write_b64 v6, v[224:225] offset:8704\n"
      "ds_write_b64 v6, v[226:227] offset:8720\n"
      "ds_write_b64 v6, v[228:229] offset:8736\n"
      "ds_write_b64 v6, v[230:231] offset:8752\n"
      "s_waitcnt lgkmcnt(0)\n"
      "s_barrier\n"
      "ds_read_b128 v[232:235], v7 offset:8704\n"
      "ds_read_b128 v[236:239], v7 offset:9792\n"
      "s_add_u32 s55, s57, 7168\n"
      "s_waitcnt lgkmcnt(1)\n"
      "buffer_store_dwordx4 v[232:235], v9, s[16:19], s57 offen sc1\n"
      "s_waitcnt lgkmcnt(0)\n"
      "buffer_store_dwordx4 v[236:239], v9, s[16:19], s55 offen sc1\n"
      "s_endpgm\n"
      ".Lmk_end:\n"
      ".p2align 12\n"
      :
      : "{s[0:1]}"(__builtin_amdgcn_kernarg_segment_ptr()), "{s2}"(blockIdx.x), "{v0}"(threadIdx.x), "i"((__attribute__((address_space(3))) char*)smem)
      : "v1","v2","v3","v4","v5","v6","v7","v8","v9","v10","v11","v12","v13","v14","v15","v16","v17","v18","v19","v20","v21","v22","v23","v24","v25","v26","v27","v28","v29","v30","v31","v32","v33","v34","v35","v36","v37","v38","v39","v40","v41","v42","v43","v44","v45","v46","v47","v48","v49","v50","v51","v52","v53","v54","v55","v56","v57","v58","v59","v60","v61","v62","v63","v64","v65","v66","v67","v68","v69","v70","v71","v72","v73","v74","v75","v76","v77","v78","v79","v80","v81","v82","v83","v84","v85","v86","v87","v88","v89","v90","v91","v92","v93","v94","v95","v96","v97","v98","v99","v100","v101","v102","v103","v104","v105","v106","v107","v108","v109","v110","v111","v112","v113","v114","v115","v116","v117","v118","v119","v120","v121","v122","v123","v124","v125","v126","v127","v128","v129","v130","v131","v132","v133","v134","v135","v136","v137","v138","v139","v140","v141","v142","v143","v144","v145","v146","v147","v148","v149","v150","v151","v152","v153","v154","v155","v156","v157","v158","v159","v160","v161","v162","v163","v164","v165","v166","v167","v168","v169","v170","v171","v172","v173","v174","v175","v176","v177","v178","v179","v180","v181","v182","v183","v184","v185","v186","v187","v188","v189","v190","v191","v192","v193","v194","v195","v196","v197","v198","v199","v200","v201","v202","v203","v204","v205","v206","v207","v208","v209","v210","v211","v212","v213","v214","v215","v216","v217","v218","v219","v220","v221","v222","v223","v224","v225","v226","v227","v228","v229","v230","v231","v232","v233","v234","v235","v236","v237","v238","v239","v240","v241","v242","v243","v244","v245","v246","v247","v248","v249","v250","v251","v252","v253","v254","v255","s3","s4","s5","s6","s7","s8","s9","s10","s11","s12","s13","s14","s15","s16","s17","s18","s19","s20","s21","s22","s23","s24","s25","s26","s27","s28","s29","s30","s31","s33","s34","s35","s36","s37","s38","s39","s40","s41","s42","s43","s44","s45","s46","s47","s48","s49","s50","s51","s52","s53","s54","s55","s56","s57","s58","s59","s60","s61","s62","s63","s64","s65","s66","s67","s68","s69","s70","s71","s72","s73","s74","s75","s76","s77","s78","s79","s80","s81","s82","s83","s84","s85","s86","s87","s88","s89","s90","s91","s92","s93","s94","s95","s96","s97","s98","s99","vcc","scc","memory");
#endif
}

extern "C" void kernel_launch(void* const* d_in, const int* in_sizes, int n_in, void* d_out, int out_size, void* d_ws, size_t ws_size, hipStream_t stream) {
  mk_kernel<<<256, 512, 0, stream>>>(d_in[3], d_in[4], d_in[0], d_in[1], d_in[2], d_in[5], d_out, d_ws);
}
```
